```python
import torch

def _softmax(x, axis=-1):
    return torch.softmax(x, dim=axis)
def _gelu(x):
    return torch.nn.functional.gelu(x, approximate="tanh")
def _silu(x):
    return torch.nn.functional.silu(x)
def _sigmoid(x):
    return torch.sigmoid(x)
def _layernorm(x, axis=-1, eps=1e-5):
    m = x.mean(dim=axis, keepdim=True)
    v = x.var(dim=axis, keepdim=True, unbiased=False)
    return (x - m) / torch.sqrt(v + eps)
def _scatter_add(tgt, idx, src, axis=0):
    return tgt.clone().index_add_(axis, idx, src)
def _segment_sum(src, seg, n):
    out = torch.zeros((n,) + src.shape[1:], dtype=src.dtype)
    return out.index_add_(0, seg, src)
def _pad(x, pw):
    flat = tuple(v for pair in reversed(pw) for v in pair)
    return torch.nn.functional.pad(x, flat)

kernel_name = 'gated_mix__grouped_kv_mqa__spectral_gfnet__prompt'

def setup_inputs():

    _w = (lambda a: (lambda t: t.to(torch.bfloat16) if t.is_floating_point() else t)(torch.as_tensor(a)))
    def _A_setup():
        B, S, D, H = 1, 2048, 512, 32
        Dh = D // H
        return {
            "x": _w((lambda *s: torch.randn(*s, dtype=torch.bfloat16) * 0.05)(B, S, D)),
            "wq": _w((lambda *s: torch.randn(*s, dtype=torch.bfloat16) * 0.8)(D, H * Dh)),
            "wk": _w((lambda *s: torch.randn(*s, dtype=torch.bfloat16) * 0.8)(D, Dh)),
            "wv": _w((lambda *s: torch.randn(*s, dtype=torch.bfloat16) * 0.05)(D, Dh)),
            "wo": _w((lambda *s: torch.randn(*s, dtype=torch.bfloat16) * 0.05)(H * Dh, D)),
        }
    _A_in = _A_setup()

    def _B_setup():
        B, S, D = 1, 2048, 512
        Df = D // 2 + 1
        return {
            "x": _w((lambda *s: torch.randn(*s, dtype=torch.bfloat16) * 0.05)(B, S, D)),
            "w_r": _w((lambda *s: torch.randn(*s, dtype=torch.bfloat16) * 0.4)(S, Df) * 0.02),
            "w_i": _w((lambda *s: torch.randn(*s, dtype=torch.bfloat16) * 0.8)(S, Df) * 0.02),
        }
    _B_in = _B_setup()

    return {
        "A_x": _A_in["x"],
        "A_wq": _A_in["wq"],
        "A_wk": _A_in["wk"],
        "A_wv": _A_in["wv"],
        "A_wo": _A_in["wo"],
        "B_x": _B_in["x"],
        "B_w_r": _B_in["w_r"],
        "B_w_i": _B_in["w_i"],
        "gate_w": _w((lambda *s: torch.randn(*s, dtype=torch.bfloat16) * 0.05)(512) * 16.0),
    }

def reference(A_x, A_wq, A_wk, A_wv, A_wo, B_x, B_w_r, B_w_i, gate_w):
    def _A_ref(x, wq, wk, wv, wo):
        B, S, D = x.shape
        H = 32
        Dh = D // H
        q = (lambda x, *d: x.permute(*d))((x @ wq).reshape(B, S, H, Dh), 0, 2, 1, 3)
        k = x @ wk
        v = x @ wv
        scores = torch.einsum("bhqd,bkd->bhqk", q, k) * (Dh ** -0.5)
        p = _softmax(scores, axis=-1)
        o = torch.einsum("bhqk,bkd->bhqd", p, v)
        return (lambda x, *d: x.permute(*d))(o, 0, 2, 1, 3).reshape(B, S, H * Dh) @ wo

    def _B_ref(x, w_r, w_i):
        B, S, D = x.shape
        X = (lambda x, axes=(-2,-1): torch.fft.rfft2(x.float(), dim=axes))(x, axes=(-2, -1))
        W = w_r + 1j * w_i
        X = X * W
        y = (lambda x, s, axes=(-2,-1): torch.fft.irfft2(x, s=s, dim=axes).to(torch.bfloat16))(X, (S, D), axes=(-2, -1))
        return y

    x = A_x
    g = _sigmoid((x * gate_w).sum(-1, keepdims=True))
    a = _A_ref(x, A_wq, A_wk, A_wv, A_wo)
    b = _B_ref(x, B_w_r, B_w_i)
    return g * a + (1 - g) * b
```

```cpp
#include <hip/hip_runtime.h>
#include <hip/hip_bf16.h>
#include <stdint.h>

typedef short    bf16x8 __attribute__((ext_vector_type(8)));
typedef short    s16x4  __attribute__((ext_vector_type(4)));
typedef float    f32x16 __attribute__((ext_vector_type(16)));
typedef float    f32x4  __attribute__((ext_vector_type(4)));
typedef float    f32x2  __attribute__((ext_vector_type(2)));
typedef unsigned u32x4  __attribute__((ext_vector_type(4)));
typedef unsigned u32x2  __attribute__((ext_vector_type(2)));
typedef __bf16   bf16x2_t __attribute__((ext_vector_type(2)));
typedef f32x2 cf;
#define DI __device__ __forceinline__
#define LAS __attribute__((address_space(3)))
#define MFMA32(a, b, c) __builtin_amdgcn_mfma_f32_32x32x16_bf16((a), (b), (c), 0, 0, 0)

constexpr int S = 2048, D = 512, H = 32, DH = 16, DF = 257;

constexpr size_t WS_Q    = 0;
constexpr size_t WS_K    = WS_Q + (size_t)S * D * 2;
constexpr size_t WS_V    = WS_K + 65536;
constexpr size_t WS_KMAX = WS_V + 65536;
constexpr size_t WS_G    = WS_KMAX + 1024;
constexpr size_t WS_X1   = WS_G + 8192;
constexpr size_t WS_WT   = WS_X1 + (size_t)256 * S * 8;
constexpr size_t WS_Z    = WS_WT + (size_t)260 * S * 4;
constexpr size_t WS_B    = WS_Z + (size_t)256 * S * 8;
constexpr size_t WS_O    = WS_B + (size_t)1024 * 512 * 4;
constexpr size_t WS_END  = WS_O + (size_t)S * D * 2;

DI unsigned pk_bf16(float lo, float hi) { f32x2 f = {lo, hi}; return __builtin_bit_cast(unsigned, __builtin_convertvector(f, bf16x2_t)); }
DI float bf_lo(unsigned u) { return __uint_as_float(u << 16); }
DI float bf_hi(unsigned u) { return __uint_as_float(u & 0xffff0000u); }
DI float bfr(float x) { return bf_lo(pk_bf16(x, x)); }

DI cf cmul(cf a, cf w)  { cf t = a.xx * w; cf u = {-w.y, w.x}; return a.yy * u + t; }
DI cf cmulc(cf a, cf w) { cf t = {a.x * w.x, a.y * w.x}; cf u = {w.y, -w.y}; return a.yx * u + t; }
template <int INV> DI cf rot90(cf a) { return INV ? cf{-a.y, a.x} : cf{a.y, -a.x}; }
template <int INV> DI cf ctw(cf a, cf w) { return INV ? cmulc(a, w) : cmul(a, w); }
template <int INV> DI void dft4(cf& c0, cf& c1, cf& c2, cf& c3) {
  cf d0 = c0 + c2, d2 = c0 - c2, d1 = c1 + c3, d3 = rot90<INV>(c1 - c3);
  c0 = d0 + d1; c2 = d0 - d1; c1 = d2 + d3; c3 = d2 - d3;
}
template <int INV> DI void dft8(cf (&a)[8]) {
  constexpr float R = 0.70710678118654752440f;
  cf b0 = a[0] + a[4], b4 = a[0] - a[4], b1 = a[1] + a[5], b5 = a[1] - a[5];
  cf b2 = a[2] + a[6], b6 = a[2] - a[6], b3 = a[3] + a[7], b7 = a[3] - a[7];
  cf r5 = rot90<INV>(b5), r7 = rot90<INV>(b7);
  b5 = (b5 + r5) * R;  b6 = rot90<INV>(b6);  b7 = (r7 - b7) * R;
  dft4<INV>(b0, b1, b2, b3);
  dft4<INV>(b4, b5, b6, b7);
  a[0] = b0; a[2] = b1; a[4] = b2; a[6] = b3;
  a[1] = b4; a[3] = b5; a[5] = b6; a[7] = b7;
}
DI cf twid(float rev) { return cf{__builtin_amdgcn_cosf(rev), -__builtin_amdgcn_sinf(rev)}; }

template <typename WS>
DI f32x16 gemm_tile(const unsigned short* __restrict__ act, int row0, WS wsrc, char* lds, int wid, int lane) {
  const int wr = wid >> 1, wn = wid & 1, r = lane & 31, h = lane >> 5;
#pragma unroll
  for (int i = 0; i < 16; ++i) {
    const int ins = wid * 16 + i, k = ins * 8 + (lane >> 3), c = (lane & 7) ^ (((k >> 1) & 1) << 2);
    __builtin_amdgcn_global_load_lds((const __attribute__((address_space(1))) unsigned*)wsrc(k, c), (LAS unsigned*)(lds + ins * 1024), 16, 0, 0);
  }
  const unsigned short* ap = act + (size_t)(row0 + 32 * wr + r) * D + 8 * h;
  bf16x8 xf[32];
#pragma unroll
  for (int t = 0; t < 32; ++t) xf[t] = *(const bf16x8*)(ap + 16 * t);
  asm volatile("s_waitcnt vmcnt(0)" ::: "memory");
  __syncthreads();
  const int g = lane >> 4, i16 = lane & 15, q = i16 >> 2, p = i16 & 3;
  const char* base = lds + (8 * h + q) * 128 + ((wn ^ (q >> 1)) * 64) + 32 * (g & 1) + 8 * p;
  f32x16 acc = {};
#pragma unroll
  for (int t = 0; t < 32; ++t) {
    s16x4 lo = __builtin_amdgcn_ds_read_tr16_b64_v4i16((LAS s16x4*)(base + t * 2048));
    s16x4 hi = __builtin_amdgcn_ds_read_tr16_b64_v4i16((LAS s16x4*)(base + t * 2048 + 512));
    bf16x8 wf = __builtin_shufflevector(lo, hi, 0, 1, 2, 3, 4, 5, 6, 7);
    acc = MFMA32(wf, xf[t], acc);
  }
  return acc;
}

template <int INV>
DI void fft512_wave(cf (&a)[8], cf* buf  , int lane) {
  dft8<INV>(a);
  {
    const float rev = (float)lane * (1.0f / 512.0f);
#pragma unroll
    for (int k = 1; k < 8; ++k) a[k] = ctw<INV>(a[k], twid(rev * k));
  }
#pragma unroll
  for (int k = 0; k < 8; ++k) buf[k * 72 + lane] = a[k];
  __builtin_amdgcn_fence(__ATOMIC_RELEASE, "wavefront"); __builtin_amdgcn_wave_barrier(); __builtin_amdgcn_fence(__ATOMIC_ACQUIRE, "wavefront");
  const int k1 = lane >> 3, n3 = lane & 7;
#pragma unroll
  for (int j = 0; j < 8; ++j) a[j] = buf[k1 * 72 + n3 + 8 * j];
  dft8<INV>(a);
  {
    const float rev = (float)n3 * (1.0f / 64.0f);
#pragma unroll
    for (int k = 1; k < 8; ++k) a[k] = ctw<INV>(a[k], twid(rev * k));
  }
  __builtin_amdgcn_fence(__ATOMIC_RELEASE, "wavefront"); __builtin_amdgcn_wave_barrier(); __builtin_amdgcn_fence(__ATOMIC_ACQUIRE, "wavefront");
#pragma unroll
  for (int k = 0; k < 8; ++k) buf[k1 * 72 + 9 * k + n3] = a[k];
  __builtin_amdgcn_fence(__ATOMIC_RELEASE, "wavefront"); __builtin_amdgcn_wave_barrier(); __builtin_amdgcn_fence(__ATOMIC_ACQUIRE, "wavefront");
#pragma unroll
  for (int j = 0; j < 8; ++j) a[j] = buf[k1 * 72 + 9 * n3 + j];
  dft8<INV>(a);
}

__global__ __launch_bounds__(256, 2) void k1_prep(const unsigned short* __restrict__ x, const unsigned short* __restrict__ wq,
                                                  const unsigned short* __restrict__ wk, const unsigned short* __restrict__ wv,
                                                  const unsigned short* __restrict__ w_r, const unsigned short* __restrict__ w_i,
                                                  const unsigned short* __restrict__ gw, char* __restrict__ ws) {
  extern __shared__ __attribute__((aligned(16))) char lds[];
  const int tid = threadIdx.x, wid = __builtin_amdgcn_readfirstlane(tid >> 6), lane = tid & 63, b = blockIdx.x;
  const int r = lane & 31, h = lane >> 5;
  if (b < 256) {
    const int rb = b >> 3, nb = b & 7;
    auto wsrc = [&](int k, int c) { return wq + (size_t)k * D + nb * 64 + c * 8; };
    f32x16 acc = gemm_tile(x, rb * 64, wsrc, lds, wid, lane);
    constexpr float C = 0.25f * 1.4426950408889634f;
    unsigned short* q = (unsigned short*)(ws + WS_Q) + (size_t)(rb * 64 + 32 * (wid >> 1) + r) * D + nb * 64 + 32 * (wid & 1) + 4 * h;
#pragma unroll
    for (int g = 0; g < 4; ++g) {
      u32x2 o = {pk_bf16(acc[4 * g] * C, acc[4 * g + 1] * C), pk_bf16(acc[4 * g + 2] * C, acc[4 * g + 3] * C)};
      *(u32x2*)(q + 8 * g) = o;
    }
  } else if (b < 288) {
    const int rb = b - 256;
    auto wsrc = [&](int k, int c) { return ((c & 2) ? wv : wk) + (size_t)k * DH + (c & 1) * 8; };
    f32x16 acc = gemm_tile(x, rb * 64, wsrc, lds, wid, lane);
    if ((wid & 1) == 0) {
      const int tile = rb * 2 + (wid >> 1);
      unsigned kp[4];
#pragma unroll
      for (int i = 0; i < 4; ++i) kp[i] = pk_bf16(acc[2 * i], acc[2 * i + 1]);
      char* kimg = ws + WS_K + tile * 1024 + r * 16 + 8 * h;
      *(u32x2*)(kimg) = u32x2{kp[0], kp[1]};
      *(u32x2*)(kimg + 512) = u32x2{kp[2], kp[3]};
      float n2 = 0.f;
#pragma unroll
      for (int i = 0; i < 4; ++i) { float a = bf_lo(kp[i]), c = bf_hi(kp[i]); n2 += a * a + c * c; }
      n2 += __shfl_xor(n2, 32);
#pragma unroll
      for (int o = 16; o >= 1; o >>= 1) n2 = fmaxf(n2, __shfl_xor(n2, o));
      if (lane == 0) ((float*)(ws + WS_KMAX))[tile] = n2;
      unsigned short* vimg = (unsigned short*)(ws + WS_V + tile * 1024 + (r >> 4) * 512 + ((r >> 2) & 1) * 256) + ((r >> 3) & 1) * 4 + (r & 3);
#pragma unroll
      for (int i = 0; i < 8; ++i) {
        const int d = (i & 3) + 8 * (i >> 2) + 4 * h;
        vimg[d * 8] = (unsigned short)(pk_bf16(acc[8 + i], acc[8 + i]) & 0xffffu);
      }
    }
  } else {
    const int rb = b - 288;
    u32x4* wl = (u32x4*)(lds + 20480);
    {
      const u32x4* gr = (const u32x4*)(w_r + (size_t)rb * 8 * DF);
      const u32x4* gi = (const u32x4*)(w_i + (size_t)rb * 8 * DF);
      wl[tid] = gr[tid]; wl[257 + tid] = gi[tid];
      if (tid == 0) { wl[256] = gr[256]; wl[257 + 256] = gi[256]; }
    }
    const int s1 = rb * 8 + wid * 2;
    const unsigned short* x1 = x + (size_t)s1 * D;
    cf a[8];
    float gs1 = 0.f, gs2 = 0.f;
#pragma unroll
    for (int j = 0; j < 8; ++j) {
      float u = __uint_as_float((unsigned)x1[lane + 64 * j] << 16), v = __uint_as_float((unsigned)x1[D + lane + 64 * j] << 16);
      float w = __uint_as_float((unsigned)gw[lane + 64 * j] << 16);
      a[j] = cf{u, v};
      unsigned pr = pk_bf16(u * w, v * w);
      gs1 += bf_lo(pr); gs2 += bf_hi(pr);
    }
#pragma unroll
    for (int o = 32; o >= 1; o >>= 1) { gs1 += __shfl_xor(gs1, o); gs2 += __shfl_xor(gs2, o); }
    if (lane < 2) {
      float z = bfr(lane ? gs2 : gs1);
      float g = 1.0f / (1.0f + __expf(-z));
      ((float*)(ws + WS_G))[s1 + lane] = bfr(g);
    }
    cf* buf = (cf*)(lds + wid * 4608 + 0);
    fft512_wave<0>(a, buf, lane);
    const int kl = (lane >> 3) + 8 * (lane & 7);
    __builtin_amdgcn_fence(__ATOMIC_RELEASE, "wavefront"); __builtin_amdgcn_wave_barrier(); __builtin_amdgcn_fence(__ATOMIC_ACQUIRE, "wavefront");
#pragma unroll
    for (int k = 0; k < 4; ++k) buf[k * 64 + kl] = a[4 + k];
    __builtin_amdgcn_fence(__ATOMIC_RELEASE, "wavefront"); __builtin_amdgcn_wave_barrier(); __builtin_amdgcn_fence(__ATOMIC_ACQUIRE, "wavefront");
    constexpr float SC = 1.0f / 2097152.0f;
    f32x4* x1t = (f32x4*)(ws + WS_X1);
#pragma unroll
    for (int k = 0; k < 4; ++k) {
      const int v = kl + 64 * k;
      cf A = a[k], B = buf[(256 - v) & 255];
      f32x4 o;
      if (v == 0) o = f32x4{2.f * A.x, 2.f * B.x, 2.f * A.y, 2.f * B.y};
      else        o = f32x4{A.x + B.x, A.y - B.y, A.y + B.y, B.x - A.x};
      x1t[((size_t)v * S + s1) >> 1] = o * SC;
    }
    __syncthreads();
    {
      const unsigned short* lr = (const unsigned short*)wl;
      const unsigned short* li = lr + 257 * 8;
      for (int v = tid; v < DF; v += 256) {
        unsigned o[8];
#pragma unroll
        for (int u = 0; u < 8; ++u) o[u] = (unsigned)lr[u * DF + v] | ((unsigned)li[u * DF + v] << 16);
        u32x4* dst = (u32x4*)(ws + WS_WT + ((size_t)v * S + rb * 8) * 4);
        dst[0] = u32x4{o[0], o[1], o[2], o[3]}; dst[1] = u32x4{o[4], o[5], o[6], o[7]};
      }
    }
  }
}

DI int pad36(int p) { return p + 4 * (p >> 5); }
__global__ __launch_bounds__(256) void k2_col(char* __restrict__ ws) {
  __shared__ __attribute__((aligned(16))) cf buf[2304];
  const int t = threadIdx.x, v = blockIdx.x, lane = t & 63;
  const cf* src = (const cf*)(ws + WS_X1) + (size_t)v * S;
  cf a[8];
#pragma unroll
  for (int j = 0; j < 8; ++j) a[j] = src[t + 256 * j];
  const int k1 = t >> 5, i = t & 31, k2 = i >> 2, ip = i & 3;
  const unsigned* wt = (const unsigned*)(ws + WS_WT);
  unsigned wv[8];
#pragma unroll
  for (int c = 0; c < 2; ++c)
#pragma unroll
    for (int k4 = 0; k4 < 4; ++k4) wv[4 * c + k4] = wt[(size_t)v * S + k1 + 8 * k2 + 64 * (2 * ip + c) + 512 * k4];
  cf twA[8], twB[8], twC[8];
#pragma unroll
  for (int k = 1; k < 8; ++k) {
    twA[k] = twid((float)(t * k) * (1.0f / 2048.0f));
    twB[k] = twid((float)(i * k) * (1.0f / 256.0f));
    twC[k] = twid((float)(ip * k) * (1.0f / 32.0f));
  }
  dft8<0>(a);
#pragma unroll
  for (int k = 1; k < 8; ++k) a[k] = cmul(a[k], twA[k]);
#pragma unroll
  for (int k = 0; k < 8; ++k) buf[pad36(k * 256 + t)] = a[k];
  __syncthreads();
#pragma unroll
  for (int j = 0; j < 8; ++j) a[j] = buf[pad36(k1 * 256 + i + 32 * j)];
  dft8<0>(a);
#pragma unroll
  for (int k = 1; k < 8; ++k) a[k] = cmul(a[k], twB[k]);
  __syncthreads();
#pragma unroll
  for (int k = 0; k < 8; ++k) buf[pad36(k1 * 256 + k * 32 + i)] = a[k];
  __syncthreads();
#pragma unroll
  for (int j = 0; j < 8; ++j) a[j] = buf[pad36(k1 * 256 + k2 * 32 + ip + 4 * j)];
  dft8<0>(a);
#pragma unroll
  for (int k = 1; k < 8; ++k) a[k] = cmul(a[k], twC[k]);
  __syncthreads();
#pragma unroll
  for (int k = 0; k < 8; ++k) buf[pad36(k1 * 256 + k2 * 32 + k * 4 + ip)] = a[k];
  __syncthreads();
#pragma unroll
  for (int j = 0; j < 8; ++j) a[j] = buf[pad36(k1 * 256 + k2 * 32 + ip * 8 + j)];
  dft4<0>(a[0], a[1], a[2], a[3]);
  dft4<0>(a[4], a[5], a[6], a[7]);
  if (v != 0) {
#pragma unroll
    for (int j = 0; j < 8; ++j) a[j] = cmul(a[j], cf{bf_lo(wv[j]), bf_hi(wv[j])});
  } else {
    __syncthreads();
#pragma unroll
    for (int j = 0; j < 8; ++j) { const int u = k1 + 8 * k2 + 64 * (2 * ip + (j >> 2)) + 512 * (j & 3); buf[u] = a[j]; }
    __syncthreads();
#pragma unroll
    for (int j = 0; j < 8; ++j) {
      const int u = k1 + 8 * k2 + 64 * (2 * ip + (j >> 2)) + 512 * (j & 3), un = (S - u) & (S - 1);
      cf cn = buf[un]; cn.y = -cn.y;
      unsigned w0n = wt[un], w1 = wt[(size_t)256 * S + u], w1n = wt[(size_t)256 * S + un];
      cf h0 = cf{bf_lo(wv[j]) + bf_lo(w0n), bf_hi(wv[j]) - bf_hi(w0n)};
      cf h1 = cf{bf_lo(w1) + bf_lo(w1n), bf_hi(w1) - bf_hi(w1n)};
      cf P = (h0 + h1) * 0.25f, M = (h0 - h1) * 0.25f;
      a[j] = cmul(a[j], P) + cmul(cn, M);
    }
  }
  dft4<1>(a[0], a[1], a[2], a[3]);
  dft4<1>(a[4], a[5], a[6], a[7]);
  __syncthreads();
#pragma unroll
  for (int j = 0; j < 8; ++j) buf[pad36(k1 * 256 + k2 * 32 + ip * 8 + j)] = a[j];
  __syncthreads();
#pragma unroll
  for (int k = 0; k < 8; ++k) a[k] = buf[pad36(k1 * 256 + k2 * 32 + k * 4 + ip)];
#pragma unroll
  for (int k = 1; k < 8; ++k) a[k] = cmulc(a[k], twC[k]);
  dft8<1>(a);
  __syncthreads();
#pragma unroll
  for (int j = 0; j < 8; ++j) buf[pad36(k1 * 256 + k2 * 32 + ip + 4 * j)] = a[j];
  __syncthreads();
#pragma unroll
  for (int k = 0; k < 8; ++k) a[k] = buf[pad36(k1 * 256 + k * 32 + i)];
#pragma unroll
  for (int k = 1; k < 8; ++k) a[k] = cmulc(a[k], twB[k]);
  dft8<1>(a);
  __syncthreads();
#pragma unroll
  for (int j = 0; j < 8; ++j) buf[pad36(k1 * 256 + i + 32 * j)] = a[j];
  __syncthreads();
#pragma unroll
  for (int k = 0; k < 8; ++k) a[k] = buf[pad36(k * 256 + t)];
#pragma unroll
  for (int k = 1; k < 8; ++k) a[k] = cmulc(a[k], twA[k]);
  dft8<1>(a);
  cf* z = (cf*)(ws + WS_Z);
#pragma unroll
  for (int j = 0; j < 8; ++j) { const int s = t + 256 * j; z[((size_t)(s >> 3) * 256 + v) * 8 + (s & 7)] = a[j]; }
  (void)lane;
}

__global__ __launch_bounds__(256) void k2_row(char* __restrict__ ws) {
  __shared__ __attribute__((aligned(16))) cf bufs[4 * 576];
  const int tid = threadIdx.x, wid = tid >> 6, lane = tid & 63, sb = blockIdx.x;
  const f32x4* z = (const f32x4*)(ws + WS_Z) + (size_t)sb * 256 * 4 + wid;
  cf a[8];
#pragma unroll
  for (int j = 0; j < 4; ++j) {
    const int v = lane + 64 * j;
    f32x4 q = z[v * 4];
    a[j] = (v == 0) ? cf{q.x, q.z} : cf{q.x - q.w, q.y + q.z};
  }
#pragma unroll
  for (int j = 4; j < 8; ++j) {
    const int v = (512 - (lane + 64 * j)) & 255;
    f32x4 q = z[v * 4];
    a[j] = (v == 0) ? cf{q.y, q.w} : cf{q.x + q.w, q.z - q.y};
  }
  fft512_wave<1>(a, bufs + wid * 576, lane);
  const int kl = (lane >> 3) + 8 * (lane & 7);
  unsigned* b2 = (unsigned*)(ws + WS_B) + (size_t)(sb * 4 + wid) * D;
#pragma unroll
  for (int k = 0; k < 8; ++k) b2[kl + 64 * k] = pk_bf16(a[k].x, a[k].y);
}

constexpr int LDS_K = 0, LDS_V = 65536, LDS_ONES = 131072, LDS_ATT = 131072 + 4096;
__global__ __launch_bounds__(512, 2) void k2_attn(char* __restrict__ ws) {
  extern __shared__ __attribute__((aligned(16))) char lds[];
  const int tid = threadIdx.x, wid = __builtin_amdgcn_readfirstlane(tid >> 6), lane = tid & 63, b = blockIdx.x;
  const int rb = b & 63, hg = b >> 6, r = lane & 31, h = lane >> 5, head = hg * 8 + wid;
#pragma unroll
  for (int i = 0; i < 16; ++i) {
    const int ins = i * 8 + wid;
    __builtin_amdgcn_global_load_lds((const __attribute__((address_space(1))) unsigned*)(ws + WS_K + ins * 1024 + lane * 16), (LAS unsigned*)(lds + ins * 1024), 16, 0, 0);
  }
  *(u32x2*)(lds + LDS_ONES + tid * 8) = u32x2{0x3f803f80u, 0x3f803f80u};
  const bf16x8 qf = *(const bf16x8*)((const unsigned short*)(ws + WS_Q) + (size_t)(rb * 32 + r) * D + head * 16 + 8 * h);
  float km = ((const float*)(ws + WS_KMAX))[lane];
#pragma unroll
  for (int o = 32; o >= 1; o >>= 1) km = fmaxf(km, __shfl_xor(km, o));
  float qn = 0.f;
  {
    u32x4 qu = __builtin_bit_cast(u32x4, qf);
#pragma unroll
    for (int i = 0; i < 4; ++i) { float a = bf_lo(qu[i]), c = bf_hi(qu[i]); qn += a * a + c * c; }
    qn += __shfl_xor(qn, 32);
  }
  float negm = -__builtin_sqrtf(qn * km) * 1.0001f;
  asm volatile("s_waitcnt vmcnt(0)" ::: "memory");
  __syncthreads();

  const char* kp = lds + LDS_K + lane * 16;
  const int vstep = (r < 16) ? 4096 : 0;
  f32x16 O;
  for (int attempt = 0; attempt < 2; ++attempt) {
    f32x16 C;
#pragma unroll
    for (int i = 0; i < 16; ++i) C[i] = negm;
    O = f32x16{};
    int va = (r < 16) ? (LDS_V + h * 256 + r * 16) : (LDS_ONES + h * 256 + (r - 16) * 16);
    for (int t4 = 0; t4 < 16; ++t4) {
#pragma unroll
      for (int u = 0; u < 4; ++u) {
        const bf16x8 kf = *(const bf16x8*)(kp + (t4 * 4 + u) * 1024);
        const bf16x8 v0 = *(const bf16x8*)(lds + va + u * 1024);
        const bf16x8 v1 = *(const bf16x8*)(lds + va + u * 1024 + 512);
        f32x16 Sx = MFMA32(kf, qf, C);
        u32x4 p0, p1;
#pragma unroll
        for (int i = 0; i < 4; ++i) p0[i] = pk_bf16(__builtin_amdgcn_exp2f(Sx[2 * i]), __builtin_amdgcn_exp2f(Sx[2 * i + 1]));
#pragma unroll
        for (int i = 0; i < 4; ++i) p1[i] = pk_bf16(__builtin_amdgcn_exp2f(Sx[8 + 2 * i]), __builtin_amdgcn_exp2f(Sx[9 + 2 * i]));
        O = MFMA32(v0, __builtin_bit_cast(bf16x8, p0), O);
        O = MFMA32(v1, __builtin_bit_cast(bf16x8, p1), O);
      }
      va += vstep;
    }
    if (attempt == 1 || __all(O[8] >= 1.2621774483536189e-29f)) break;
    float m = -__builtin_inff();
    const f32x16 Z = {};
    for (int t = 0; t < 64; ++t) {
      const bf16x8 kf = *(const bf16x8*)(kp + t * 1024);
      f32x16 Sx = MFMA32(kf, qf, Z);
#pragma unroll
      for (int i = 0; i < 16; ++i) m = fmaxf(m, Sx[i]);
    }
    m = fmaxf(m, __shfl_xor(m, 32));
    negm = -m;
  }
  const float inv = 1.0f / O[8];
  unsigned short* o = (unsigned short*)(ws + WS_O) + (size_t)(rb * 32 + r) * D + head * 16 + 4 * h;
  *(u32x2*)(o)     = u32x2{pk_bf16(O[0] * inv, O[1] * inv), pk_bf16(O[2] * inv, O[3] * inv)};
  *(u32x2*)(o + 8) = u32x2{pk_bf16(O[4] * inv, O[5] * inv), pk_bf16(O[6] * inv, O[7] * inv)};
}

__global__ __launch_bounds__(256, 2) void k3_out(const unsigned short* __restrict__ wo, const char* __restrict__ ws, unsigned short* __restrict__ out) {
  extern __shared__ __attribute__((aligned(16))) char lds[];
  const int tid = threadIdx.x, wid = __builtin_amdgcn_readfirstlane(tid >> 6), lane = tid & 63, b = blockIdx.x;
  const int r = lane & 31, h = lane >> 5, rb = b >> 3, nb = b & 7;
  const int row = rb * 64 + 32 * (wid >> 1) + r, n0 = nb * 64 + 32 * (wid & 1) + 4 * h;
  const float g = ((const float*)(ws + WS_G))[row];
  u32x4 bb[4];
#pragma unroll
  for (int q = 0; q < 4; ++q) bb[q] = *(const u32x4*)((const unsigned*)(ws + WS_B) + (size_t)(row >> 1) * D + n0 + 8 * q);
  auto wsrc = [&](int k, int c) { return wo + (size_t)k * D + nb * 64 + c * 8; };
  f32x16 acc = gemm_tile((const unsigned short*)(ws + WS_O), rb * 64, wsrc, lds, wid, lane);
  const float omg = bfr(1.0f - g);
  const bool odd = row & 1;
#pragma unroll
  for (int q = 0; q < 4; ++q) {
    float o[4];
#pragma unroll
    for (int i = 0; i < 4; ++i) {
      const float bv = odd ? bf_hi(bb[q][i]) : bf_lo(bb[q][i]);
      o[i] = bfr(g * bfr(acc[4 * q + i])) + bfr(omg * bv);
    }
    *(u32x2*)(out + (size_t)row * D + n0 + 8 * q) = u32x2{pk_bf16(o[0], o[1]), pk_bf16(o[2], o[3])};
  }
}

extern "C" void kernel_launch(void* const* d_in, const int* in_sizes, int n_in, void* d_out, int out_size, void* d_ws, size_t ws_size,
                              hipStream_t stream) {
  const unsigned short* x  = (const unsigned short*)d_in[0];
  const unsigned short* wq = (const unsigned short*)d_in[1];
  const unsigned short* wk = (const unsigned short*)d_in[2];
  const unsigned short* wv = (const unsigned short*)d_in[3];
  const unsigned short* wo = (const unsigned short*)d_in[4];
  const unsigned short* wr = (const unsigned short*)d_in[6];
  const unsigned short* wi = (const unsigned short*)d_in[7];
  const unsigned short* gw = (const unsigned short*)d_in[8];
  char* ws = (char*)d_ws;
  (void)hipFuncSetAttribute((const void*)k1_prep, hipFuncAttributeMaxDynamicSharedMemorySize, 65536);
  (void)hipFuncSetAttribute((const void*)k2_attn, hipFuncAttributeMaxDynamicSharedMemorySize, LDS_ATT);
  (void)hipFuncSetAttribute((const void*)k3_out, hipFuncAttributeMaxDynamicSharedMemorySize, 65536);
  k1_prep<<<544, 256, 65536, stream>>>(x, wq, wk, wv, wr, wi, gw, ws);
  k2_col<<<256, 256, 0, stream>>>(ws);
  k2_row<<<256, 256, 0, stream>>>(ws);
  k2_attn<<<256, 512, LDS_ATT, stream>>>(ws);
  k3_out<<<256, 256, 65536, stream>>>(wo, ws, (unsigned short*)d_out);
}
```

```cpp
#include <hip/hip_runtime.h>
#include <hip/hip_bf16.h>
#include <stdint.h>

typedef short    bf16x8 __attribute__((ext_vector_type(8)));
typedef short    s16x4  __attribute__((ext_vector_type(4)));
typedef float    f32x16 __attribute__((ext_vector_type(16)));
typedef float    f32x4  __attribute__((ext_vector_type(4)));
typedef float    f32x2  __attribute__((ext_vector_type(2)));
typedef unsigned u32x4  __attribute__((ext_vector_type(4)));
typedef unsigned u32x2  __attribute__((ext_vector_type(2)));
typedef __bf16   bf16x2_t __attribute__((ext_vector_type(2)));
typedef f32x2 cf;
typedef unsigned short u16;
#define DI __device__ __forceinline__
#define LAS __attribute__((address_space(3)))
#define GAS __attribute__((address_space(1)))
#define MFMA32(a, b, c) __builtin_amdgcn_mfma_f32_32x32x16_bf16((a), (b), (c), 0, 0, 0)
#define MFMA16(a, b, c) __builtin_amdgcn_mfma_f32_16x16x32_bf16((a), (b), (c), 0, 0, 0)
#define SB() __builtin_amdgcn_sched_barrier(0)
#define MEMBAR() asm volatile("" ::: "memory")

constexpr int S = 2048, D = 512, DF = 257;

constexpr size_t WS_CNT  = 0;
constexpr size_t WS_K    = 16384;
constexpr size_t WS_V    = WS_K + 65536;
constexpr size_t WS_KP   = WS_V + 65536;
constexpr size_t WS_G    = WS_KP + 4096;
constexpr size_t WS_PM   = WS_G + 8192;
constexpr size_t WS_Q    = WS_PM + 32768;
constexpr size_t WS_X1   = WS_Q + (size_t)S * D * 2;
constexpr size_t WT_STRIDE = 8192 + 256;
constexpr size_t WS_WT   = WS_X1 + (size_t)256 * S * 8;
constexpr size_t Z_STRIDE = 16384 + 256;
constexpr size_t WS_Z    = WS_WT + 256 * WT_STRIDE;
constexpr size_t WS_B    = WS_Z + 256 * Z_STRIDE;
constexpr size_t WS_O    = WS_B + (size_t)S * D * 2;
constexpr size_t WS_WO   = WS_O + (size_t)S * D * 2;
constexpr size_t WS_END  = WS_WO + (size_t)D * D * 2;
constexpr unsigned SENT = 0xFFFFFFFFu;

#define STAMP(k, slot) do {} while (0)

DI unsigned pk_bf16(float lo, float hi) { f32x2 f = {lo, hi}; return __builtin_bit_cast(unsigned, __builtin_convertvector(f, bf16x2_t)); }
DI float bf_lo(unsigned u) { return __uint_as_float(u << 16); }
DI float bf_hi(unsigned u) { return __uint_as_float(u & 0xffff0000u); }
DI float bf_f(u16 u) { return __uint_as_float((unsigned)u << 16); }
DI float bfr(float x) { return bf_lo(pk_bf16(x, x)); }

DI void dma16(const void* sbase, unsigned voff, unsigned lds_addr) {
  unsigned keep;
  asm volatile("s_mov_b32 %0, m0\n\ts_mov_b32 m0, %3\n\ts_nop 4\n\tglobal_load_lds_dwordx4 %1, %2\n\ts_mov_b32 m0, %0"
               : "=&s"(keep) : "v"(voff), "s"(sbase), "s"(lds_addr) : "memory");
}
template <int N> DI void wait_vm() { asm volatile("s_waitcnt vmcnt(%0)" :: "i"(N) : "memory"); }
DI void lds_signal(unsigned addr, int lane) {
  asm volatile("s_waitcnt lgkmcnt(0)" ::: "memory");
  if (lane == 0) __hip_atomic_fetch_add((LAS unsigned*)addr, 1u, __ATOMIC_RELAXED, __HIP_MEMORY_SCOPE_WORKGROUP);
  MEMBAR();
}
DI void lds_wait(unsigned addr, unsigned n) {
  MEMBAR();
  while ((unsigned)__builtin_amdgcn_readfirstlane(__hip_atomic_load((LAS unsigned*)addr, __ATOMIC_RELAXED, __HIP_MEMORY_SCOPE_WORKGROUP)) < n) __builtin_amdgcn_s_sleep(1);
  MEMBAR();
}
DI void lds_wait_slow(unsigned addr, unsigned n) {
  MEMBAR();
  while ((unsigned)__builtin_amdgcn_readfirstlane(__hip_atomic_load((LAS unsigned*)addr, __ATOMIC_RELAXED, __HIP_MEMORY_SCOPE_WORKGROUP)) < n) __builtin_amdgcn_s_sleep(6);
  MEMBAR();
}
DI void wave_sync() { __builtin_amdgcn_fence(__ATOMIC_RELEASE, "wavefront"); __builtin_amdgcn_wave_barrier(); __builtin_amdgcn_fence(__ATOMIC_ACQUIRE, "wavefront"); }
DI void st16_dev(void* p, u32x4 v) { asm volatile("global_store_dwordx4 %0, %1, off sc1\n\ts_nop 1" :: "v"(p), "v"(v) : "memory"); }

#define K1_SC1 1
#define ST16(p, v) st16_dev((void*)(p), (v))
template <int CTRL> DI float dppf(float x) { return __int_as_float(__builtin_amdgcn_update_dpp(0, __float_as_int(x), CTRL, 0xf, 0xf, true)); }
DI float x16_sum(float x) { auto r = __builtin_amdgcn_permlane16_swap(__float_as_uint(x), __float_as_uint(x), false, false); return __uint_as_float(r[0]) + __uint_as_float(r[1]); }
DI float x32_sum(float x) { auto r = __builtin_amdgcn_permlane32_swap(__float_as_uint(x), __float_as_uint(x), false, false); return __uint_as_float(r[0]) + __uint_as_float(r[1]); }
DI float x16_max(float x) { auto r = __builtin_amdgcn_permlane16_swap(__float_as_uint(x), __float_as_uint(x), false, false); return fmaxf(__uint_as_float(r[0]), __uint_as_float(r[1])); }
DI float x32_max(float x) { auto r = __builtin_amdgcn_permlane32_swap(__float_as_uint(x), __float_as_uint(x), false, false); return fmaxf(__uint_as_float(r[0]), __uint_as_float(r[1])); }
DI float row16_sum(float x) { x += dppf<0xB1>(x); x += dppf<0x4E>(x); x += dppf<0x141>(x); x += dppf<0x140>(x); return x; }
DI float row16_max(float x) { x = fmaxf(x, dppf<0xB1>(x)); x = fmaxf(x, dppf<0x4E>(x)); x = fmaxf(x, dppf<0x141>(x)); x = fmaxf(x, dppf<0x140>(x)); return x; }
DI float wave_sum(float x) { return x32_sum(x16_sum(row16_sum(x))); }
DI float wave_max(float x) { return x32_max(x16_max(row16_max(x))); }
DI bool wave_all(bool p) { return __builtin_amdgcn_ballot_w64(!p) == 0ull; }

DI cf cmul(cf a, cf w)  { cf t = a.xx * w; cf u = {-w.y, w.x}; return a.yy * u + t; }
DI cf cmulc(cf a, cf w) { cf t = a * w.xx; cf u = {w.y, -w.y}; return a.yx * u + t; }
template <int INV> DI cf rot90(cf a) { return INV ? cf{-a.y, a.x} : cf{a.y, -a.x}; }
template <int INV> DI cf ctw(cf a, cf w) { return INV ? cmulc(a, w) : cmul(a, w); }
template <int INV> DI void dft4(cf& c0, cf& c1, cf& c2, cf& c3) {
  cf d0 = c0 + c2, d2 = c0 - c2, d1 = c1 + c3, d3 = rot90<INV>(c1 - c3);
  c0 = d0 + d1; c2 = d0 - d1; c1 = d2 + d3; c3 = d2 - d3;
}
template <int INV> DI void dft8(cf (&a)[8]) {
  constexpr float R = 0.70710678118654752440f;
  cf b0 = a[0] + a[4], b4 = a[0] - a[4], b1 = a[1] + a[5], b5 = a[1] - a[5];
  cf b2 = a[2] + a[6], b6 = a[2] - a[6], b3 = a[3] + a[7], b7 = a[3] - a[7];
  cf r5 = rot90<INV>(b5), r7 = rot90<INV>(b7);
  b5 = (b5 + r5) * R;  b6 = rot90<INV>(b6);  b7 = (r7 - b7) * R;
  dft4<INV>(b0, b1, b2, b3);
  dft4<INV>(b4, b5, b6, b7);
  a[0] = b0; a[2] = b1; a[4] = b2; a[6] = b3;
  a[1] = b4; a[3] = b5; a[5] = b6; a[7] = b7;
}
DI cf twid(float rev) { return cf{__builtin_amdgcn_cosf(rev), -__builtin_amdgcn_sinf(rev)}; }
DI cf csq(cf a) { return cf{a.x * a.x - a.y * a.y, 2.f * a.x * a.y}; }
DI void twid7(cf (&w)[8], float rev) {
  w[1] = twid(rev); w[2] = csq(w[1]); w[3] = cmul(w[2], w[1]); w[4] = csq(w[2]); w[5] = cmul(w[4], w[1]); w[6] = csq(w[3]); w[7] = cmul(w[4], w[3]);
}

template <int INV>
DI void fft512_wave(cf (&a)[8], LAS cf* buf  , int lane) {
  const int k1 = lane >> 3, n3 = lane & 7;
  dft8<INV>(a);
  {
    cf w[8]; twid7(w, (float)lane * (1.0f / 512.0f));
#pragma unroll
    for (int k = 1; k < 8; ++k) a[k] = ctw<INV>(a[k], w[k]);
  }
#pragma unroll
  for (int k = 0; k < 8; ++k) buf[k * 72 + lane] = a[k];
  wave_sync();
#pragma unroll
  for (int j = 0; j < 8; ++j) a[j] = buf[k1 * 72 + n3 + 8 * j];
  dft8<INV>(a);
  {
    cf w[8]; twid7(w, (float)n3 * (1.0f / 64.0f));
#pragma unroll
    for (int k = 1; k < 8; ++k) a[k] = ctw<INV>(a[k], w[k]);
  }
  wave_sync();
#pragma unroll
  for (int k = 0; k < 8; ++k) buf[k1 * 72 + 9 * k + n3] = a[k];
  wave_sync();
#pragma unroll
  for (int j = 0; j < 8; ++j) a[j] = buf[k1 * 72 + 9 * n3 + j];
  dft8<INV>(a);
}

constexpr unsigned K1_X = 0, K1_W = 65536, K1_FFT = 131072, K1_KV = K1_FFT + 18432, K1_WT = K1_KV + 4096, K1_SYNC = K1_WT + 9216, K1_LDS = K1_SYNC + 64;
__attribute__((target("no-packed-fp32-ops"))) __global__ __launch_bounds__(768, 3) void k1_prep(const u16* __restrict__ x, const u16* __restrict__ wq, const u16* __restrict__ wk,
                                                  const u16* __restrict__ wv, const u16* __restrict__ wo, const u16* __restrict__ w_r,
                                                  const u16* __restrict__ w_i, const u16* __restrict__ gw, char* __restrict__ ws) {
  extern __shared__ __attribute__((aligned(16))) char lds_[];
  LAS char* lds = (LAS char*)lds_;
  const unsigned lb = (unsigned)(uintptr_t)lds;
  const int tid = threadIdx.x, wid = __builtin_amdgcn_readfirstlane(tid >> 6), lane = tid & 63, b = blockIdx.x;
  const int rb = (b & 7) * 4 + (b >> 6), nb = (b >> 3) & 7;
  const int r = lane & 31, h = lane >> 5;
  STAMP(0, wid >> 2);
  if (tid < 16) *(LAS unsigned*)(lds + K1_SYNC + tid * 4) = 0u;
  asm volatile("s_waitcnt lgkmcnt(0)" ::: "memory");
  __builtin_amdgcn_s_barrier();
  MEMBAR();
  if (wid < 4) {
    {
      unsigned vx[4];
#pragma unroll
      for (int i = 0; i < 4; ++i) { const int rr = 4 * i + (lane >> 4); vx[i] = (unsigned)(rr * 1024 + (((lane & 15) ^ rr) << 4)); }
      const unsigned vw = (unsigned)((lane >> 3) * 1024 + (((lane & 7) ^ (((lane >> 4) & 1) << 2)) << 4));
      const char* xb = (const char*)x + (size_t)(rb * 64 + wid * 16) * 1024;
      const char* wb = (const char*)wq + (size_t)(wid * 32) * 1024 + nb * 128;
#pragma unroll
      for (int c = 0; c < 4; ++c) {
#pragma unroll
        for (int i = 0; i < 4; ++i) dma16(xb + c * 256, vx[i], lb + K1_X + c * 16384 + (wid * 4 + i) * 1024);
#pragma unroll
        for (int i = 0; i < 4; ++i) dma16(wb + (size_t)(c * 128 + i * 8) * 1024, vw, lb + K1_W + c * 16384 + (wid * 4 + i) * 1024);
      }
    }
    const int wr = wid >> 1, wn = wid & 1, row = 32 * wr + r;
    const int g = lane >> 4, q = (lane & 15) >> 2, p = lane & 3;
    LAS char* wbase = lds + K1_W + (8 * h + q) * 128 + ((wn ^ (q >> 1)) * 64) + 32 * (g & 1) + 8 * p;
    LAS char* xbase = lds + K1_X + row * 256;
    const int xs = (h ^ (row & 15)) << 4;
    f32x16 acc = {};
#pragma unroll
    for (int c = 0; c < 4; ++c) {
      if (c == 0) wait_vm<24>(); else if (c == 1) wait_vm<16>(); else if (c == 2) wait_vm<8>(); else wait_vm<0>();
      lds_signal(lb + K1_SYNC + c * 4, lane);
      lds_wait(lb + K1_SYNC + c * 4, 4);
#pragma unroll
      for (int t = 0; t < 8; ++t) {
        const bf16x8 xf = *(LAS bf16x8*)(xbase + c * 16384 + (xs ^ (t << 5)));
        s16x4 lo = __builtin_amdgcn_ds_read_tr16_b64_v4i16((LAS s16x4*)(wbase + (c * 8 + t) * 2048));
        s16x4 hi = __builtin_amdgcn_ds_read_tr16_b64_v4i16((LAS s16x4*)(wbase + (c * 8 + t) * 2048 + 512));
        acc = MFMA32(__builtin_shufflevector(lo, hi, 0, 1, 2, 3, 4, 5, 6, 7), xf, acc);
      }
    }
    constexpr float C = 0.25f * 1.4426950408889634f;
    char* qd = ws + WS_Q + ((size_t)((rb * 2 + wr) * 32 + nb * 4 + wn * 2) * 64 + lane) * 16;
#pragma unroll
    for (int hh = 0; hh < 2; ++hh) {
      u32x4 o;
#pragma unroll
      for (int i = 0; i < 4; ++i) o[i] = pk_bf16(acc[8 * hh + 2 * i] * C, acc[8 * hh + 2 * i + 1] * C);
      ST16(qd + hh * 1024, o);
    }
    STAMP(0, 3);
  } else if (wid < 8) {
    __builtin_amdgcn_s_setprio(3);
    const int w = wid - 4, ft = tid - 256, sA = b + 512 * w;
    const u32x4 xa = *(const u32x4*)(x + (size_t)sA * D + lane * 8), xb = *(const u32x4*)(x + (size_t)(sA + 256) * D + lane * 8);
    const u32x4 gq = *(const u32x4*)(gw + lane * 8);
    const u16* kvs = (nb < 4 ? wk : wv) + 4 * (nb & 3);
    const u32x2 kv0 = *(const u32x2*)(kvs + ft * 16), kv1 = *(const u32x2*)(kvs + (ft + 256) * 16);
    LAS cf* buf = (LAS cf*)(lds + K1_FFT + w * 4608);
    *(LAS u32x4*)((LAS char*)buf + lane * 16) = xa;
    *(LAS u32x4*)((LAS char*)buf + 1024 + lane * 16) = xb;
    wave_sync();
    cf a[8];
#pragma unroll
    for (int j = 0; j < 8; ++j) a[j] = cf{bf_f(((LAS u16*)buf)[lane + 64 * j]), bf_f(((LAS u16*)buf)[512 + lane + 64 * j])};
    wave_sync();
    fft512_wave<0>(a, buf, lane);
    const int kl = (lane >> 3) + 8 * (lane & 7);
    wave_sync();
#pragma unroll
    for (int k = 0; k < 4; ++k) buf[k * 64 + kl] = a[4 + k];
    wave_sync();
    f32x4 sep[4];
#pragma unroll
    for (int k = 0; k < 4; ++k) {
      const int v = kl + 64 * k;
      const cf A = a[k], B = buf[(256 - v) & 255];
      if (v == 0) sep[k] = f32x4{2.f * A.x, 2.f * B.x, 2.f * A.y, 2.f * B.y};
      else        sep[k] = f32x4{A.x + B.x, A.y - B.y, A.y + B.y, B.x - A.x};
    }
    wave_sync();
#pragma unroll
    for (int k = 0; k < 4; ++k) ((LAS f32x4*)buf)[kl + 64 * k] = sep[k];
    *(LAS u32x2*)(lds + K1_KV + ft * 8) = kv0;
    *(LAS u32x2*)(lds + K1_KV + (ft + 256) * 8) = kv1;
    lds_signal(lb + K1_SYNC + 16, lane);
    {
      float gs1 = 0.f, gs2 = 0.f;
#pragma unroll
      for (int i = 0; i < 4; ++i) {
        const unsigned pa = pk_bf16(bf_lo(xa[i]) * bf_lo(gq[i]), bf_hi(xa[i]) * bf_hi(gq[i])), pb = pk_bf16(bf_lo(xb[i]) * bf_lo(gq[i]), bf_hi(xb[i]) * bf_hi(gq[i]));
        gs1 += bf_lo(pa) + bf_hi(pa); gs2 += bf_lo(pb) + bf_hi(pb);
      }
      gs1 = wave_sum(gs1); gs2 = wave_sum(gs2);
      if (lane < 2) {
        const float z = bfr(lane ? gs2 : gs1);
        ((float*)(ws + WS_G))[sA + 256 * lane] = bfr(__builtin_amdgcn_rcpf(1.0f + __builtin_amdgcn_exp2f(z * -1.4426950408889634f)));
      }
    }
    cf tw[8]; twid7(tw, (float)b * (1.0f / 2048.0f));
    constexpr float SC = 1.0f / 2097152.0f;
    lds_wait(lb + K1_SYNC + 16, 4);
#pragma unroll
    for (int q = 0; q < 4; ++q) {
      const f32x4 e = *(LAS f32x4*)(lds + K1_FFT + q * 4608 + ft * 16) * SC;
      a[2 * q] = cf{e.x, e.y}; a[2 * q + 1] = cf{e.z, e.w};
    }
    lds_signal(lb + K1_SYNC + 20, lane);
    dft8<0>(a);
#pragma unroll
    for (int k = 1; k < 8; ++k) a[k] = cmul(a[k], tw[k]);
    lds_wait(lb + K1_SYNC + 20, 4);
#pragma unroll
    for (int q = 0; q < 4; ++q) ((LAS f32x4*)buf)[lane * 4 + (q ^ ((lane >> 1) & 3))] = f32x4{a[2 * q].x, a[2 * q].y, a[2 * q + 1].x, a[2 * q + 1].y};
    wave_sync();
    {
      char* xo = ws + WS_X1 + (size_t)b * 16384 + w * 4096 + lane * 16;
#pragma unroll
      for (int m = 0; m < 4; ++m) { const int p = m * 64 + lane; ST16(xo + m * 1024, ((LAS u32x4*)buf)[p ^ ((p >> 3) & 3)]); }
    }
    STAMP(0, 4);
    lds_wait(lb + K1_SYNC + 12, 4);
    {
      const int g = lane >> 4, q = (lane & 15) >> 2, row = 16 * w + (lane & 15);
      LAS char* ab = lds + K1_KV + (8 * g + q) * 8;
      LAS char* xbse = lds + K1_X + row * 256;
      const int xs = (g ^ (row & 15)) << 4;
      f32x4 acc = {};
#pragma unroll
      for (int t = 0; t < 16; ++t) {
        const bf16x8 xf = *(LAS bf16x8*)(xbse + (t >> 2) * 16384 + (xs ^ ((t & 3) << 6)));
        s16x4 lo = __builtin_amdgcn_ds_read_tr16_b64_v4i16((LAS s16x4*)(ab + t * 256));
        s16x4 hi = __builtin_amdgcn_ds_read_tr16_b64_v4i16((LAS s16x4*)(ab + t * 256 + 32));
        acc = MFMA16(__builtin_shufflevector(lo, hi, 0, 1, 2, 3, 4, 5, 6, 7), xf, acc);
      }
      const unsigned p0 = pk_bf16(acc[0], acc[1]), p1 = pk_bf16(acc[2], acc[3]);
      const int key = rb * 64 + row, tile = key >> 5, kr = key & 31;
      if (nb < 4) {
        float n2 = bf_lo(p0) * bf_lo(p0) + bf_hi(p0) * bf_hi(p0) + bf_lo(p1) * bf_lo(p1) + bf_hi(p1) * bf_hi(p1);
        n2 = row16_max(n2);
        if (lane < 16) *(u32x2*)(ws + WS_K + tile * 1024 + (nb & 1) * 512 + kr * 16 + (nb >> 1) * 8) = u32x2{p0, p1};
        if (lane == 0) ((float*)(ws + WS_KP))[(rb * 4 + w) * 4 + nb] = n2;
      } else if (lane < 16) {
        u16* vi = (u16*)(ws + WS_V + tile * 1024 + (kr >> 4) * 512 + ((kr >> 2) & 1) * 256 + (nb - 4) * 64) + ((kr >> 3) & 1) * 4 + (kr & 3);
        vi[0] = (u16)p0; vi[8] = (u16)(p0 >> 16); vi[16] = (u16)p1; vi[24] = (u16)(p1 >> 16);
      }
    }
    STAMP(0, 5);
  } else {
    const int mt = tid - 512, mw = wid - 8;
    const int ub = (b >> 5) + 8 * ((b >> 2) & 7), vb = (b & 3) * 64 + mw * 16;
    typedef unsigned u32x4_u __attribute__((ext_vector_type(4), aligned(2)));
    const size_t wofs = (size_t)(ub + 64 * (lane >> 1)) * DF + vb + (lane & 1) * 8;
    const u32x4 fr = *(const u32x4_u*)(w_r + wofs), fi = *(const u32x4_u*)(w_i + wofs);
    u16 wov[8];
    const int frag = 2 * b + (mw & 1);
    if (mw < 2) {
#pragma unroll
      for (int j = 0; j < 8; ++j) wov[j] = wo[(16 * (frag & 31) + 8 * (j >> 2) + 4 * h + (j & 3)) * D + 32 * (frag >> 5) + r];
    }
    float pmv[8];
    if (mt >= 128 && mt < 136) {
      const int j = mt - 128, u = ub + 128 * (b & 3) + 64 * (j >> 2) + 512 * (j & 3), un = (S - u) & (S - 1);
      pmv[0] = bf_f(w_r[u * DF]); pmv[1] = bf_f(w_i[u * DF]); pmv[2] = bf_f(w_r[un * DF]); pmv[3] = bf_f(w_i[un * DF]);
      pmv[4] = bf_f(w_r[u * DF + 256]); pmv[5] = bf_f(w_i[u * DF + 256]); pmv[6] = bf_f(w_r[un * DF + 256]); pmv[7] = bf_f(w_i[un * DF + 256]);
    }
    if (mt == 192) {
      if (b < 8) *(unsigned*)(ws + WS_CNT + b * 256) = 0u;
      if (b < 64) *(unsigned*)(ws + WS_CNT + 4096 + b * 64) = 0u;
    }
    {
      LAS char* st = lds + K1_WT + mw * 2304;
      LAS unsigned* d0 = (LAS unsigned*)(st + (lane >> 1) * 36 + (lane & 1) * 16);
#pragma unroll
      for (int i = 0; i < 4; ++i) { d0[i] = fr[i]; d0[288 + i] = fi[i]; }
      wave_sync();
      const int q = lane & 7;
#pragma unroll
      for (int n = 0; n < 2; ++n) {
        const int vv = (lane >> 3) + 8 * n;
        const LAS u16* sr = (const LAS u16*)(st + q * 36) + vv;
        u32x4 o;
#pragma unroll
        for (int i = 0; i < 4; ++i) o[i] = (unsigned)sr[i * 144] | ((unsigned)sr[576 + i * 144] << 16);
        ST16(ws + WS_WT + (size_t)(vb + vv) * WT_STRIDE + (b >> 2) * 128 + q * 16, o);
      }
    }
    if (mw < 2) {
      u32x4 o;
#pragma unroll
      for (int j = 0; j < 4; ++j) o[j] = (unsigned)wov[2 * j] | ((unsigned)wov[2 * j + 1] << 16);
      ST16(ws + WS_WO + (size_t)frag * 1024 + lane * 16, o);
    } else if (mt < 136) {
      const cf h0 = {pmv[0] + pmv[2], pmv[1] - pmv[3]}, h1 = {pmv[4] + pmv[6], pmv[5] - pmv[7]};
      const cf P = (h0 + h1) * 0.25f, M = (h0 - h1) * 0.25f;
      ((f32x4*)(ws + WS_PM))[b * 8 + (mt - 128)] = f32x4{P.x, P.y, M.x, M.y};
    }
    STAMP(0, 6);
  }
}

constexpr unsigned L_K = 0, L_V = 65536, L_ONES = 131072, L_ZERO = L_ONES + 2048, L_FFT = L_ZERO + 2048, L_SYNC = L_FFT + 18688, K2_LDS = L_SYNC + 256;
constexpr unsigned SY_KV0 = 0, SY_KV1 = 4, SY_FB = 8  , SY_PUB = 12, SY_GO = 16, SY_FFTDONE = 20, SY_ODONE = 24, SY_OSH = 28,
                   SY_TICK = 32  , SY_BPUB = 64, SY_BGO = 68;
#define EX(i) Sc[i] = __builtin_amdgcn_exp2f(Sc[i]); SB()
#define CV(P, j, i) P[j] = pk_bf16(Sc[i], Sc[i + 1]); SB()
DI void att_step(f32x16& Sc, f32x16& Sn, f32x16& Op, f32x16& Oc, const f32x16& C, const bf16x8& qn, const bf16x8& kn, const bf16x8& v1p, const bf16x8& v0c,
                 u32x4& p1) {
  u32x4 p0;
  Sn = MFMA32(kn, qn, C); SB();
  EX(0); EX(1); EX(2); EX(3); CV(p0, 0, 0); EX(4); CV(p0, 1, 2); EX(5);
  Op = MFMA32(v1p, __builtin_bit_cast(bf16x8, p1), Op); SB();
  EX(6); EX(7); CV(p0, 2, 4); EX(8); CV(p0, 3, 6); EX(9); EX(10);
  Oc = MFMA32(v0c, __builtin_bit_cast(bf16x8, p0), Oc); SB();
  EX(11); CV(p1, 0, 8); EX(12); EX(13); CV(p1, 1, 10); EX(14); EX(15); CV(p1, 2, 12); CV(p1, 3, 14);
}
constexpr unsigned SPIN_CAP = 1u << 22;

__attribute__((target("no-packed-fp32-ops"))) __global__ __launch_bounds__(768, 3) void k2_main(char* __restrict__ ws, u16* __restrict__ out) {
  extern __shared__ __attribute__((aligned(16))) char lds_[];
  LAS char* lds = (LAS char*)lds_;
  const unsigned lb = (unsigned)(uintptr_t)lds;
  const int tid = threadIdx.x, wid = __builtin_amdgcn_readfirstlane(tid >> 6), lane = tid & 63, b = blockIdx.x;
  const int rb = b & 63, hg = b >> 6, r = lane & 31, h = lane >> 5;
  if (wid < 8) {
    STAMP(1, 0);
    const int hp = wid & 3, kh = wid >> 2, head = hg * 8 + hp * 2;
    bf16x8 qA, qB; f32x4 kp0, kp1;
    {
      const char* qp = ws + WS_Q + ((size_t)(rb * 32 + head) * 64 + lane) * 16;
      const char* kq = ws + WS_KP + lane * 16;
      asm volatile("global_load_dwordx4 %0, %4, off\n\tglobal_load_dwordx4 %1, %4, off offset:1024\n\tglobal_load_dwordx4 %2, %5, off\n\tglobal_load_dwordx4 %3, %5, off offset:1024"
                   : "=&v"(qA), "=&v"(qB), "=&v"(kp0), "=&v"(kp1) : "v"(qp), "v"(kq) : "memory");
    }
#pragma unroll
    for (int i = 0; i < 4; ++i) { const int p = i * 8 + wid, off = ((p & 3) * 32 + (p >> 2)) * 1024; dma16(ws + WS_K + off, lane * 16, lb + off); }
    { const unsigned c = tid < 256 ? 0x3f803f80u : 0u; *(LAS u32x2*)(lds + L_ONES + tid * 8) = u32x2{c, c}; }
    asm volatile("s_waitcnt lgkmcnt(0)" ::: "memory");
    __builtin_amdgcn_s_barrier();
    asm volatile("s_waitcnt vmcnt(4)" : "+v"(qA), "+v"(qB), "+v"(kp0), "+v"(kp1) :: "memory");
    float negm;
    {
      float km = fmaxf(kp0[0] + kp0[1] + kp0[2] + kp0[3], kp1[0] + kp1[1] + kp1[2] + kp1[3]);
      km = wave_max(km);
      const u32x4 qu = __builtin_bit_cast(u32x4, qA), qv = __builtin_bit_cast(u32x4, qB);
      float na = 0.f, nq = 0.f;
#pragma unroll
      for (int i = 0; i < 4; ++i) { float a = bf_lo(qu[i]), c = bf_hi(qu[i]); na += a * a + c * c; a = bf_lo(qv[i]); c = bf_hi(qv[i]); nq += a * a + c * c; }
      na = x32_sum(na); nq = x32_sum(nq);
      negm = -__builtin_amdgcn_sqrtf(fmaxf(na, nq) * km) * 1.0001f;
    }
    LAS char* kp = lds + L_K + lane * 16;
    const int vstep = (r < 16) ? 2048 : 0;
    const int vconst = ((r & 27) == 16) ? L_ONES : L_ZERO;
    f32x16 OA = {}, OB = {};
    wait_vm<0>();
    lds_signal(lb + L_SYNC + SY_KV0, lane);
    lds_wait(lb + L_SYNC + SY_KV0, 8);
    STAMP(1, 1);
    {
      f32x16 C;
#pragma unroll
      for (int i = 0; i < 16; ++i) C[i] = negm;
      int va = (r < 16) ? (L_V + kh * 32768 + h * 256 + r * 16) : (vconst + h * 256 + (r - 16) * 16);
      int ka = kh * 32768;
#define LDK(o) (*(LAS bf16x8*)(kp + ka + (o)))
#define LDV(o) (*(LAS bf16x8*)(lds + va + (o)))
      bf16x8 k0 = LDK(0), k1, v00 = LDV(0), v10 = LDV(512), v01, v11 = v10;
      f32x16 Sa = MFMA32(k0, qA, C), Sb;
      u32x4 p1 = {0u, 0u, 0u, 0u};
#pragma unroll 1
      for (int ph = 0; ph < 2; ++ph) {
        const int nit = ph ? 13 : 3;
#pragma unroll 1
        for (int it = 0; it < nit; ++it) {
          k1 = LDK(1024); v01 = LDV(1024); SB();
          att_step(Sa, Sb, OB, OA, C, qB, k0, v11, v00, p1);
          v11 = LDV(1024 + 512); SB();
          att_step(Sb, Sa, OA, OB, C, qA, k1, v10, v00, p1);
          va += vstep;
          k0 = LDK(2048); v00 = LDV(0); SB();
          att_step(Sa, Sb, OB, OA, C, qB, k1, v10, v01, p1);
          v10 = LDV(512); SB();
          att_step(Sb, Sa, OA, OB, C, qA, k0, v11, v01, p1);
          ka += 2048;
        }
        if (ph == 0) lds_wait(lb + L_SYNC + SY_KV1, 4);
      }
      OB = MFMA32(v11, __builtin_bit_cast(bf16x8, p1), OB);
    }
    STAMP(1, 2 + kh);
    LAS float* red = (LAS float*)(lds + L_FFT + hp * 4608) + lane;
    unsigned tk = 0;
    if (lane == 0) tk = __hip_atomic_fetch_add((LAS unsigned*)(lds + L_SYNC + SY_TICK + hp * 8), 1u, __ATOMIC_RELAXED, __HIP_MEMORY_SCOPE_WORKGROUP);
    tk = __builtin_amdgcn_readfirstlane(tk);
    if (tk == 0) {
      lds_wait(lb + L_SYNC + SY_FFTDONE, 4);
#pragma unroll
      for (int i = 0; i < 9; ++i) { red[i * 64] = OA[i]; red[(9 + i) * 64] = OB[i]; }
      lds_signal(lb + L_SYNC + SY_TICK + hp * 8 + 4, lane);
      return;
    }
    lds_wait(lb + L_SYNC + SY_TICK + hp * 8 + 4, 1);
#pragma unroll
    for (int i = 0; i < 9; ++i) { OA[i] += red[i * 64]; OB[i] += red[(9 + i) * 64]; }
    if (!wave_all(OA[8] >= 1.2621774483536189e-29f && OB[8] >= 1.2621774483536189e-29f)) {
#pragma unroll 1
      for (int hh = 0; hh < 2; ++hh) {
        const bf16x8 qf = hh ? qB : qA;
        float m = -__builtin_inff();
        f32x16 C = {};
#pragma unroll 1
        for (int t = 0; t < 64; ++t) {
          f32x16 Sx = MFMA32(*(LAS bf16x8*)(kp + t * 1024), qf, C);
#pragma unroll
          for (int i = 0; i < 16; ++i) m = fmaxf(m, Sx[i]);
        }
        m = x32_max(m);
#pragma unroll
        for (int i = 0; i < 16; ++i) C[i] = -m;
        f32x16 O = {};
        int va = (r < 16) ? (L_V + h * 256 + r * 16) : (vconst + h * 256 + (r - 16) * 16);
#pragma unroll 1
        for (int t = 0; t < 64; ++t) {
          f32x16 Sx = MFMA32(*(LAS bf16x8*)(kp + t * 1024), qf, C);
          u32x4 p0, p1;
#pragma unroll
          for (int i = 0; i < 4; ++i) {
            p0[i] = pk_bf16(__builtin_amdgcn_exp2f(Sx[2 * i]), __builtin_amdgcn_exp2f(Sx[2 * i + 1]));
            p1[i] = pk_bf16(__builtin_amdgcn_exp2f(Sx[8 + 2 * i]), __builtin_amdgcn_exp2f(Sx[9 + 2 * i]));
          }
          O = MFMA32(LDV(0), __builtin_bit_cast(bf16x8, p0), O);
          O = MFMA32(LDV(512), __builtin_bit_cast(bf16x8, p1), O);
          va += vstep >> 1;
        }
        if (hh) OB = O; else OA = O;
      }
    }
    {
      const float ia = __builtin_amdgcn_rcpf(OA[8]), ib = __builtin_amdgcn_rcpf(OB[8]);
      u32x4 fa, fb;
#pragma unroll
      for (int i = 0; i < 4; ++i) {
        fa[i] = pk_bf16(OA[2 * i] * ia, OA[2 * i + 1] * ia); fb[i] = pk_bf16(OB[2 * i] * ib, OB[2 * i + 1] * ib);
        if (fa[i] == SENT) fa[i] = 0x7fc07fc0u;
        if (fb[i] == SENT) fb[i] = 0x7fc07fc0u;
      }
      char* og = ws + WS_O + ((size_t)(rb * 32 + head) * 64 + lane) * 16;
      st16_dev(og, fa); st16_dev(og + 1024, fb);
      wave_sync();
      *(LAS u32x4*)(lds + L_FFT + hp * 4608 + lane * 16) = fa;
      *(LAS u32x4*)(lds + L_FFT + hp * 4608 + 1024 + lane * 16) = fb;
      lds_signal(lb + L_SYNC + SY_ODONE, lane);
    }
    STAMP(1, 4);
    return;
  }
  STAMP(2, 0);
  const int w = wid - 8, t = tid - 512;
  if (w == 0) *(LAS unsigned*)(lds + L_SYNC + lane * 4) = 0u;
  asm volatile("s_waitcnt lgkmcnt(0)" ::: "memory");
  __builtin_amdgcn_s_barrier();
  MEMBAR();
  __builtin_amdgcn_s_setprio(2);
  LAS cf* buf = (LAS cf*)(lds + L_FFT);
  unsigned fbn = 0;
#define FFT_BAR() do { fbn += 4; lds_signal(lb + L_SYNC + SY_FB, lane); lds_wait(lb + L_SYNC + SY_FB, fbn); } while (0)
  {
    const int v = (b & 7) * 32 + (b >> 3);
    const int k1n = lane & 7, in_ = (lane >> 3) + 8 * w;
    const int k1 = t >> 5, k2 = (t >> 2) & 7, ip = t & 3;
    cf a[8]; u32x4 wv0, wv1;
    {
      const unsigned xo = (unsigned)((in_ * 256 + v) * 64 + k1n * 8);
      const char* xb = ws + WS_X1;
#define LDX(j) asm volatile("global_load_dwordx2 %0, %1, %2" : "=&v"(a[j]) : "v"(xo), "s"(xb + (size_t)(j) * 524288) : "memory")
      LDX(0); LDX(4); LDX(1); LDX(5); LDX(2); LDX(6); LDX(3); LDX(7);
      const char* wt = ws + WS_WT + (size_t)v * WT_STRIDE;
      asm volatile("global_load_dwordx4 %0, %2, %3\n\tglobal_load_dwordx4 %1, %2, %3 offset:16" : "=&v"(wv0), "=&v"(wv1) : "v"(t * 32), "s"(wt) : "memory");
    }
    cf twA[8], twB[8], twC[8];
    twid7(twB, (float)in_ * (1.0f / 256.0f));
    twid7(twC, (float)ip * (1.0f / 32.0f));
    twid7(twA, (float)t * (1.0f / 2048.0f));
    LAS cf* bN = buf + k1n * 292 + in_;
    LAS cf* bO = buf + k1 * 292 + k2 * 36 + ip;
    LAS cf* bP = buf + k1 * 292 + k2 * 36 + ip * 8;
    LAS cf* bA = buf + (t >> 5) * 36 + (t & 31);
    asm volatile("s_waitcnt vmcnt(0)" : "+v"(a[0]), "+v"(a[1]), "+v"(a[2]), "+v"(a[3]), "+v"(a[4]), "+v"(a[5]), "+v"(a[6]), "+v"(a[7]), "+v"(wv0), "+v"(wv1) :: "memory");
#pragma unroll
    for (int i = 0; i < 24; ++i) { const int off = (w * 32 + 8 + i) * 1024; dma16(ws + WS_K + off, lane * 16, lb + off); }
    dft8<0>(a);
#pragma unroll
    for (int k = 1; k < 8; ++k) a[k] = cmul(a[k], twB[k]);
#pragma unroll
    for (int k = 0; k < 8; ++k) bN[36 * k] = a[k];
    FFT_BAR();
#pragma unroll
    for (int j = 0; j < 8; ++j) a[j] = bO[4 * j];
    dft8<0>(a);
#pragma unroll
    for (int k = 1; k < 8; ++k) a[k] = cmul(a[k], twC[k]);
#pragma unroll
    for (int k = 0; k < 8; ++k) bO[4 * k] = a[k];
    wave_sync();
#pragma unroll
    for (int j = 0; j < 8; ++j) a[j] = bP[j];
    wait_vm<0>();
    lds_signal(lb + L_SYNC + SY_KV1, lane);
    {
      char* of = ws + WS_O + ((size_t)rb * 32 + hg * 8) * 1024 + t * 16;
      st16_dev(of, u32x4{SENT, SENT, SENT, SENT}); st16_dev(of + 4096, u32x4{SENT, SENT, SENT, SENT});
    }
    dft4<0>(a[0], a[1], a[2], a[3]);
    dft4<0>(a[4], a[5], a[6], a[7]);
    if (v != 0) {
#pragma unroll
      for (int j = 0; j < 4; ++j) { a[j] = cmul(a[j], cf{bf_lo(wv0[j]), bf_hi(wv0[j])}); a[4 + j] = cmul(a[4 + j], cf{bf_lo(wv1[j]), bf_hi(wv1[j])}); }
    } else {
      f32x4 pm[8];
#pragma unroll
      for (int j = 0; j < 8; ++j) pm[j] = ((const f32x4*)(ws + WS_PM))[t * 8 + j];
      FFT_BAR();
#pragma unroll
      for (int j = 0; j < 8; ++j) buf[k1 + 8 * k2 + 64 * (2 * ip + (j >> 2)) + 512 * (j & 3)] = a[j];
      FFT_BAR();
#pragma unroll
      for (int j = 0; j < 8; ++j) {
        const int u = k1 + 8 * k2 + 64 * (2 * ip + (j >> 2)) + 512 * (j & 3);
        cf cn = buf[(S - u) & (S - 1)]; cn.y = -cn.y;
        a[j] = cmul(a[j], cf{pm[j].x, pm[j].y}) + cmul(cn, cf{pm[j].z, pm[j].w});
      }
      FFT_BAR();
    }
    dft4<1>(a[0], a[1], a[2], a[3]);
    dft4<1>(a[4], a[5], a[6], a[7]);
#pragma unroll
    for (int j = 0; j < 8; ++j) bP[j] = a[j];
    wave_sync();
#pragma unroll
    for (int k = 0; k < 8; ++k) a[k] = bO[4 * k];
#pragma unroll
    for (int k = 1; k < 8; ++k) a[k] = cmulc(a[k], twC[k]);
    dft8<1>(a);
#pragma unroll
    for (int j = 0; j < 8; ++j) bO[4 * j] = a[j];
    FFT_BAR();
#pragma unroll
    for (int k = 0; k < 8; ++k) a[k] = bN[36 * k];
#pragma unroll
    for (int k = 1; k < 8; ++k) a[k] = cmulc(a[k], twB[k]);
    dft8<1>(a);
#pragma unroll
    for (int j = 0; j < 8; ++j) bN[36 * j] = a[j];
    FFT_BAR();
#pragma unroll
    for (int k = 0; k < 8; ++k) a[k] = bA[292 * k];
#pragma unroll
    for (int k = 1; k < 8; ++k) a[k] = cmulc(a[k], twA[k]);
    dft8<1>(a);
#pragma unroll
    for (int j = 0; j < 8; ++j) bA[292 * j] = a[j];
    wave_sync();
    {
      const int te = t & ~1, jo = (t & 1) * 4;
      char* zb = ws + WS_Z + (size_t)((te >> 3) + jo * 32) * Z_STRIDE + v * 64 + (te & 7) * 8;
      const LAS cf* src = bA - (t & 1) + 292 * jo;
#pragma unroll
      for (int j = 0; j < 4; ++j) st16_dev(zb + (size_t)j * 32 * Z_STRIDE, *(const LAS u32x4*)(src + 292 * j));
    }
    wait_vm<0>();
    unsigned last = 0;
    if (lane == 0) last = __hip_atomic_fetch_add((LAS unsigned*)(lds + L_SYNC + SY_PUB), 1u, __ATOMIC_RELAXED, __HIP_MEMORY_SCOPE_WORKGROUP);
    last = __builtin_amdgcn_readfirstlane(last);
    STAMP(2, 1);
    if (last == 3) {
      if (lane == 0) __hip_atomic_fetch_add((GAS unsigned*)(ws + WS_CNT + (b & 7) * 256), 1u, __ATOMIC_RELAXED, __HIP_MEMORY_SCOPE_AGENT);
      for (unsigned spin = 0; spin < SPIN_CAP; ++spin) {
        const unsigned c = __hip_atomic_load((GAS unsigned*)(ws + WS_CNT + (lane & 7) * 256), __ATOMIC_RELAXED, __HIP_MEMORY_SCOPE_AGENT);
        if (wave_all(c == 32u)) break;
        __builtin_amdgcn_s_sleep(4);
      }
      lds_signal(lb + L_SYNC + SY_GO, lane);
    }
    lds_wait_slow(lb + L_SYNC + SY_GO, 1);
  }
  STAMP(2, 2);
  {
    const int sblk = rb * 4 + hg;
    cf a[8];
    {
      const char* z = ws + WS_Z + (size_t)sblk * Z_STRIDE + w * 16;
      const unsigned o1 = lane * 64, o2 = ((64 - lane) & 63) * 64 + (lane ? 0 : 4096);
      f32x4 q[8];
      asm volatile("global_load_dwordx4 %0, %8, %10 sc1\n\tglobal_load_dwordx4 %1, %8, %11 sc1\n\tglobal_load_dwordx4 %2, %8, %12 sc1\n\tglobal_load_dwordx4 %3, %8, %13 sc1\n\t"
                   "global_load_dwordx4 %4, %9, %13 sc1\n\tglobal_load_dwordx4 %5, %9, %12 sc1\n\tglobal_load_dwordx4 %6, %9, %11 sc1\n\tglobal_load_dwordx4 %7, %9, %10 sc1\n\ts_waitcnt vmcnt(0)"
                   : "=&v"(q[0]), "=&v"(q[1]), "=&v"(q[2]), "=&v"(q[3]), "=&v"(q[4]), "=&v"(q[5]), "=&v"(q[6]), "=&v"(q[7])
                   : "v"(o1), "v"(o2), "s"(z), "s"(z + 4096), "s"(z + 8192), "s"(z + 12288) : "memory");
#pragma unroll
      for (int j = 0; j < 4; ++j) a[j] = cf{q[j].x - q[j].w, q[j].y + q[j].z};
#pragma unroll
      for (int j = 4; j < 8; ++j) a[j] = cf{q[j].x + q[j].w, q[j].z - q[j].y};
      if (lane == 0) { a[0] = cf{q[0].x, q[0].z}; a[4] = cf{q[0].y, q[0].w}; }
    }
    LAS cf* wbuf = buf + w * 576;
    fft512_wave<1>(a, wbuf, lane);
    const int kl = (lane >> 3) + 8 * (lane & 7);
    wave_sync();
    LAS u16* rows = (LAS u16*)wbuf;
#pragma unroll
    for (int k = 0; k < 8; ++k) {
      const unsigned p = pk_bf16(a[k].x, a[k].y);
      rows[kl + 64 * k] = (u16)p; rows[520 + kl + 64 * k] = (u16)(p >> 16);
    }
    wave_sync();
    {
      const int par = lane >> 5, nt = (lane >> 1) & 15, hh = lane & 1;
      const LAS u16* src = rows + par * 520 + nt * 32 + hh * 4;
      u32x2 q[4];
#pragma unroll
      for (int g = 0; g < 4; ++g) q[g] = *(const LAS u32x2*)(src + 8 * g);
      char* bd = ws + WS_B + (((size_t)rb * 16 + nt) * 64 + hh * 32 + hg * 8 + w * 2 + par) * 32;
      st16_dev(bd, u32x4{q[0][0], q[0][1], q[1][0], q[1][1]});
      st16_dev(bd + 16, u32x4{q[2][0], q[2][1], q[3][0], q[3][1]});
    }
    lds_signal(lb + L_SYNC + SY_FFTDONE, lane);
    wait_vm<0>();
    unsigned last = 0;
    if (lane == 0) last = __hip_atomic_fetch_add((LAS unsigned*)(lds + L_SYNC + SY_BPUB), 1u, __ATOMIC_RELAXED, __HIP_MEMORY_SCOPE_WORKGROUP);
    last = __builtin_amdgcn_readfirstlane(last);
    if (last == 3) {
      GAS unsigned* cnt = (GAS unsigned*)(ws + WS_CNT + 4096 + rb * 64);
      if (lane == 0) __hip_atomic_fetch_add(cnt, 1u, __ATOMIC_RELAXED, __HIP_MEMORY_SCOPE_AGENT);
      for (unsigned spin = 0; spin < SPIN_CAP; ++spin) {
        const unsigned c = __builtin_amdgcn_readfirstlane(__hip_atomic_load(cnt, __ATOMIC_RELAXED, __HIP_MEMORY_SCOPE_AGENT));
        if (c == 4u) break;
        __builtin_amdgcn_s_sleep(4);
      }
      lds_signal(lb + L_SYNC + SY_BGO, lane);
    }
  }
  STAMP(2, 3);
  __builtin_amdgcn_s_setprio(0);
  {
    const int nt = hg * 4 + w;
    const bf16x8* wof = (const bf16x8*)(ws + WS_WO + (size_t)nt * 32768) + lane;
    bf16x8 wf[32];
#pragma unroll
    for (int k = 0; k < 32; ++k) wf[k] = wof[(((hg * 8) + k) & 31) * 64];
    const float g = ((const float*)(ws + WS_G))[rb * 32 + r];
    lds_wait_slow(lb + L_SYNC + SY_BGO, 1);
    u32x4 b0, b1;
    asm volatile("global_load_dwordx4 %0, %2, %3 sc1\n\tglobal_load_dwordx4 %1, %2, %3 offset:16 sc1\n\ts_waitcnt vmcnt(0)"
                 : "=&v"(b0), "=&v"(b1) : "v"(lane * 32), "s"(ws + WS_B + ((size_t)rb * 16 + nt) * 2048) : "memory");
    STAMP(2, 4);
    lds_wait_slow(lb + L_SYNC + SY_ODONE, 3);
    lds_wait(lb + L_SYNC + SY_ODONE, 4);
    __builtin_amdgcn_s_setprio(2);
    STAMP(2, 5);
    f32x16 acc = {};
#pragma unroll
    for (int k = 0; k < 8; ++k) acc = MFMA32(wf[k], *(LAS bf16x8*)(lds + L_FFT + (k >> 1) * 4608 + (k & 1) * 1024 + lane * 16), acc);
    {
      u32x4 f[6];
      const char* og = ws + WS_O + (size_t)rb * 32768 + lane * 16;
      const char* a0 = og + (((hg * 8) + 8 + 6 * w + 0) & 31) * 1024; const char* a1 = og + (((hg * 8) + 8 + 6 * w + 1) & 31) * 1024;
      const char* a2 = og + (((hg * 8) + 8 + 6 * w + 2) & 31) * 1024; const char* a3 = og + (((hg * 8) + 8 + 6 * w + 3) & 31) * 1024;
      const char* a4 = og + (((hg * 8) + 8 + 6 * w + 4) & 31) * 1024; const char* a5 = og + (((hg * 8) + 8 + 6 * w + 5) & 31) * 1024;
      for (unsigned spin = 0; spin < SPIN_CAP; ++spin) {
        asm volatile("global_load_dwordx4 %0, %6, off sc1\n\tglobal_load_dwordx4 %1, %7, off sc1\n\tglobal_load_dwordx4 %2, %8, off sc1\n\t"
                     "global_load_dwordx4 %3, %9, off sc1\n\tglobal_load_dwordx4 %4, %10, off sc1\n\tglobal_load_dwordx4 %5, %11, off sc1\n\ts_waitcnt vmcnt(0)"
                     : "=&v"(f[0]), "=&v"(f[1]), "=&v"(f[2]), "=&v"(f[3]), "=&v"(f[4]), "=&v"(f[5])
                     : "v"(a0), "v"(a1), "v"(a2), "v"(a3), "v"(a4), "v"(a5) : "memory");
        bool ok = true;
#pragma unroll
        for (int i = 0; i < 6; ++i) ok = ok && f[i][0] != SENT && f[i][1] != SENT && f[i][2] != SENT && f[i][3] != SENT;
        if (wave_all(ok)) break;
        __builtin_amdgcn_s_sleep(2);
      }
#pragma unroll
      for (int i = 0; i < 6; ++i) *(LAS u32x4*)(lds + L_K + (6 * w + i) * 1024 + lane * 16) = f[i];
      lds_signal(lb + L_SYNC + SY_OSH, lane);
      lds_wait(lb + L_SYNC + SY_OSH, 4);
    }
    STAMP(2, 6);
#pragma unroll
    for (int k = 8; k < 32; ++k) acc = MFMA32(wf[k], *(LAS bf16x8*)(lds + L_K + (k - 8) * 1024 + lane * 16), acc);
    const float omg = bfr(1.0f - g);
    u16* op = out + (size_t)(rb * 32 + r) * D + nt * 32 + 4 * h;
#pragma unroll
    for (int q = 0; q < 4; ++q) {
      const unsigned u0 = q < 2 ? b0[2 * q] : b1[2 * q - 4], u1 = q < 2 ? b0[2 * q + 1] : b1[2 * q - 3];
      const unsigned a01 = pk_bf16(acc[4 * q], acc[4 * q + 1]), a23 = pk_bf16(acc[4 * q + 2], acc[4 * q + 3]);
      const unsigned g01 = pk_bf16(g * bf_lo(a01), g * bf_hi(a01)), g23 = pk_bf16(g * bf_lo(a23), g * bf_hi(a23));
      const unsigned t01 = pk_bf16(omg * bf_lo(u0), omg * bf_hi(u0)), t23 = pk_bf16(omg * bf_lo(u1), omg * bf_hi(u1));
      *(u32x2*)(op + 8 * q) = u32x2{pk_bf16(bf_lo(g01) + bf_lo(t01), bf_hi(g01) + bf_hi(t01)), pk_bf16(bf_lo(g23) + bf_lo(t23), bf_hi(g23) + bf_hi(t23))};
    }
    STAMP(2, 7);
  }
}

extern "C" void kernel_launch(void* const* d_in, const int* in_sizes, int n_in, void* d_out, int out_size, void* d_ws, size_t ws_size,
                              hipStream_t stream) {
  const u16* x  = (const u16*)d_in[0];
  const u16* wq = (const u16*)d_in[1];
  const u16* wk = (const u16*)d_in[2];
  const u16* wv = (const u16*)d_in[3];
  const u16* wo = (const u16*)d_in[4];
  const u16* wr = (const u16*)d_in[6];
  const u16* wi = (const u16*)d_in[7];
  const u16* gw = (const u16*)d_in[8];
  char* ws = (char*)d_ws;
  (void)hipFuncSetAttribute((const void*)k1_prep, hipFuncAttributeMaxDynamicSharedMemorySize, K1_LDS);
  (void)hipFuncSetAttribute((const void*)k2_main, hipFuncAttributeMaxDynamicSharedMemorySize, K2_LDS);
  k1_prep<<<256, 768, K1_LDS, stream>>>(x, wq, wk, wv, wo, wr, wi, gw, ws);
  k2_main<<<256, 768, K2_LDS, stream>>>(ws, (u16*)d_out);
}
```

```cpp
#include <hip/hip_runtime.h>
#include <hip/hip_bf16.h>
#include <stdint.h>

typedef short    bf16x8 __attribute__((ext_vector_type(8)));
typedef short    s16x4  __attribute__((ext_vector_type(4)));
typedef float    f32x16 __attribute__((ext_vector_type(16)));
typedef float    f32x4  __attribute__((ext_vector_type(4)));
typedef float    f32x2  __attribute__((ext_vector_type(2)));
typedef unsigned u32x4  __attribute__((ext_vector_type(4)));
typedef unsigned u32x2  __attribute__((ext_vector_type(2)));
typedef __bf16   bf16x2_t __attribute__((ext_vector_type(2)));
typedef f32x2 cf;
typedef unsigned short u16;
#define DI __device__ __forceinline__
#define LAS __attribute__((address_space(3)))
#define GAS __attribute__((address_space(1)))
#define MFMA32(a, b, c) __builtin_amdgcn_mfma_f32_32x32x16_bf16((a), (b), (c), 0, 0, 0)
#define MFMA16(a, b, c) __builtin_amdgcn_mfma_f32_16x16x32_bf16((a), (b), (c), 0, 0, 0)
#define SB() __builtin_amdgcn_sched_barrier(0)
#define MEMBAR() asm volatile("" ::: "memory")

constexpr int S = 2048, D = 512, DF = 257;

constexpr size_t WS_EP   = 0;
constexpr size_t WS_FL   = 4096;
constexpr size_t WS_K    = 4096 + 65536;
constexpr size_t WS_V    = WS_K + 65536;
constexpr size_t WS_KP   = WS_V + 65536;
constexpr size_t WS_G    = WS_KP + 4096;
constexpr size_t WS_PM   = WS_G + 8192;
constexpr size_t WS_X1   = WS_PM + 32768;
constexpr size_t WT_STRIDE = 8192 + 256;
constexpr size_t WS_WT   = WS_X1 + (size_t)256 * S * 8;
constexpr size_t Z_STRIDE = 16384 + 256;
constexpr size_t WS_Z    = WS_WT + 256 * WT_STRIDE;
constexpr size_t WS_B    = WS_Z + 256 * Z_STRIDE;
constexpr size_t WS_O    = WS_B + (size_t)S * D * 2;
constexpr size_t WS_WO   = WS_O + (size_t)S * D * 2;
constexpr size_t WS_END  = WS_WO + (size_t)D * D * 2;
constexpr unsigned SENT = 0xFFFFFFFFu;

#define STAMP(k, slot) do {} while (0)
#define TS(i) do {} while (0)
#define TDECL do {} while (0)
#define TDUMP(k) do {} while (0)

DI unsigned pk_bf16(float lo, float hi) { f32x2 f = {lo, hi}; return __builtin_bit_cast(unsigned, __builtin_convertvector(f, bf16x2_t)); }
DI float bf_lo(unsigned u) { return __uint_as_float(u << 16); }
DI float bf_hi(unsigned u) { return __uint_as_float(u & 0xffff0000u); }
DI float bf_f(u16 u) { return __uint_as_float((unsigned)u << 16); }
DI float bfr(float x) { return bf_lo(pk_bf16(x, x)); }

DI void dma16x4(const void* sbase, unsigned v0, unsigned v1, unsigned v2, unsigned v3, unsigned lds_addr) {
  asm volatile("s_mov_b32 m0, %5\n\ts_nop 0\n\tglobal_load_lds_dwordx4 %0, %4\n\tglobal_load_lds_dwordx4 %1, %4 offset:1024\n\t"
               "global_load_lds_dwordx4 %2, %4 offset:2048\n\tglobal_load_lds_dwordx4 %3, %4 offset:3072"
               :: "v"(v0), "v"(v1), "v"(v2), "v"(v3), "s"(sbase), "s"(lds_addr) : "memory");
}
DI void dma16x4p(const void* sbase, unsigned v0, unsigned v1, unsigned v2, unsigned v3, unsigned lds_addr) {
  asm volatile("s_mov_b32 m0, %5\n\ts_nop 0\n\tglobal_load_lds_dwordx4 %0, %4\n\tglobal_load_lds_dwordx4 %1, %4 offset:1040\n\t"
               "global_load_lds_dwordx4 %2, %4 offset:2080\n\tglobal_load_lds_dwordx4 %3, %4 offset:3120"
               :: "v"(v0), "v"(v1), "v"(v2), "v"(v3), "s"(sbase), "s"(lds_addr) : "memory");
}
template <int N> DI void wait_vm() { asm volatile("s_waitcnt vmcnt(%0)" :: "i"(N) : "memory"); }
DI void lds_signal(unsigned addr, int lane) {
  asm volatile("s_waitcnt lgkmcnt(0)" ::: "memory");
  if (lane == 0) __hip_atomic_fetch_add((LAS unsigned*)addr, 1u, __ATOMIC_RELAXED, __HIP_MEMORY_SCOPE_WORKGROUP);
  MEMBAR();
}
DI void lds_wait(unsigned addr, unsigned n) {
  MEMBAR();
  while ((unsigned)__builtin_amdgcn_readfirstlane(__hip_atomic_load((LAS unsigned*)addr, __ATOMIC_RELAXED, __HIP_MEMORY_SCOPE_WORKGROUP)) < n) __builtin_amdgcn_s_sleep(1);
  MEMBAR();
}
DI void lds_wait_slow(unsigned addr, unsigned n) {
  MEMBAR();
  while ((unsigned)__builtin_amdgcn_readfirstlane(__hip_atomic_load((LAS unsigned*)addr, __ATOMIC_RELAXED, __HIP_MEMORY_SCOPE_WORKGROUP)) < n) __builtin_amdgcn_s_sleep(6);
  MEMBAR();
}
DI void wave_sync() { __builtin_amdgcn_fence(__ATOMIC_RELEASE, "wavefront"); __builtin_amdgcn_wave_barrier(); __builtin_amdgcn_fence(__ATOMIC_ACQUIRE, "wavefront"); }
DI void st16_dev(void* p, u32x4 v) { asm volatile("global_store_dwordx4 %0, %1, off sc1\n\ts_nop 1" :: "v"(p), "v"(v) : "memory"); }

DI void st8_dev(void* p, u32x2 v) { asm volatile("global_store_dwordx2 %0, %1, off sc1" :: "v"(p), "v"(v) : "memory"); }
DI void st4_dev(void* p, unsigned v) { asm volatile("global_store_dword %0, %1, off sc1" :: "v"(p), "v"(v) : "memory"); }
DI void dma16x4_dev(const void* sbase, unsigned v0, unsigned lds_addr) {
  asm volatile("s_mov_b32 m0, %2\n\ts_nop 0\n\tglobal_load_lds_dwordx4 %0, %1 sc1\n\tglobal_load_lds_dwordx4 %0, %1 offset:1024 sc1\n\t"
               "global_load_lds_dwordx4 %0, %1 offset:2048 sc1\n\tglobal_load_lds_dwordx4 %0, %1 offset:3072 sc1"
               :: "v"(v0), "s"(sbase), "s"(lds_addr) : "memory");
}
DI unsigned lds_ticket(unsigned addr, int lane) {
  unsigned t = 0;
  asm volatile("s_waitcnt lgkmcnt(0)" ::: "memory");
  if (lane == 0) t = __hip_atomic_fetch_add((LAS unsigned*)addr, 1u, __ATOMIC_RELAXED, __HIP_MEMORY_SCOPE_WORKGROUP);
  MEMBAR();
  return __builtin_amdgcn_readfirstlane(t);
}
template <int CTRL> DI float dppf(float x) { return __int_as_float(__builtin_amdgcn_update_dpp(0, __float_as_int(x), CTRL, 0xf, 0xf, true)); }
DI float x16_sum(float x) { auto r = __builtin_amdgcn_permlane16_swap(__float_as_uint(x), __float_as_uint(x), false, false); return __uint_as_float(r[0]) + __uint_as_float(r[1]); }
DI float x32_sum(float x) { auto r = __builtin_amdgcn_permlane32_swap(__float_as_uint(x), __float_as_uint(x), false, false); return __uint_as_float(r[0]) + __uint_as_float(r[1]); }
DI float x16_max(float x) { auto r = __builtin_amdgcn_permlane16_swap(__float_as_uint(x), __float_as_uint(x), false, false); return fmaxf(__uint_as_float(r[0]), __uint_as_float(r[1])); }
DI float x32_max(float x) { auto r = __builtin_amdgcn_permlane32_swap(__float_as_uint(x), __float_as_uint(x), false, false); return fmaxf(__uint_as_float(r[0]), __uint_as_float(r[1])); }
DI float row16_sum(float x) { x += dppf<0xB1>(x); x += dppf<0x4E>(x); x += dppf<0x141>(x); x += dppf<0x140>(x); return x; }
DI float row16_max(float x) { x = fmaxf(x, dppf<0xB1>(x)); x = fmaxf(x, dppf<0x4E>(x)); x = fmaxf(x, dppf<0x141>(x)); x = fmaxf(x, dppf<0x140>(x)); return x; }
DI float wave_sum(float x) { return x32_sum(x16_sum(row16_sum(x))); }
DI float wave_max(float x) { return x32_max(x16_max(row16_max(x))); }
DI unsigned umax(unsigned a, unsigned b) { return a > b ? a : b; }
DI bool wave_all(bool p) { return __builtin_amdgcn_ballot_w64(!p) == 0ull; }

DI cf cmul(cf a, cf w)  { cf t = a.xx * w; cf u = {-w.y, w.x}; return a.yy * u + t; }
DI cf cmulc(cf a, cf w) { cf t = a * w.xx; cf u = {w.y, -w.y}; return a.yx * u + t; }
template <int INV> DI cf rot90(cf a) { return INV ? cf{-a.y, a.x} : cf{a.y, -a.x}; }
template <int INV> DI cf ctw(cf a, cf w) { return INV ? cmulc(a, w) : cmul(a, w); }
template <int INV> DI void dft4(cf& c0, cf& c1, cf& c2, cf& c3) {
  cf d0 = c0 + c2, d2 = c0 - c2, d1 = c1 + c3, d3 = rot90<INV>(c1 - c3);
  c0 = d0 + d1; c2 = d0 - d1; c1 = d2 + d3; c3 = d2 - d3;
}
template <int INV> DI void dft8(cf (&a)[8]) {
  constexpr float R = 0.70710678118654752440f;
  cf b0 = a[0] + a[4], b4 = a[0] - a[4], b1 = a[1] + a[5], b5 = a[1] - a[5];
  cf b2 = a[2] + a[6], b6 = a[2] - a[6], b3 = a[3] + a[7], b7 = a[3] - a[7];
  cf r5 = rot90<INV>(b5), r7 = rot90<INV>(b7);
  b5 = (b5 + r5) * R;  b6 = rot90<INV>(b6);  b7 = (r7 - b7) * R;
  dft4<INV>(b0, b1, b2, b3);
  dft4<INV>(b4, b5, b6, b7);
  a[0] = b0; a[2] = b1; a[4] = b2; a[6] = b3;
  a[1] = b4; a[3] = b5; a[5] = b6; a[7] = b7;
}
DI cf twid(float rev) { return cf{__builtin_amdgcn_cosf(rev), -__builtin_amdgcn_sinf(rev)}; }
DI cf csq(cf a) { return cf{a.x * a.x - a.y * a.y, 2.f * a.x * a.y}; }
DI void twid7(cf (&w)[8], float rev) {
  w[1] = twid(rev); w[2] = csq(w[1]); w[3] = cmul(w[2], w[1]); w[4] = csq(w[2]); w[5] = cmul(w[4], w[1]); w[6] = csq(w[3]); w[7] = cmul(w[4], w[3]);
}

template <int INV>
DI void fft512_wave(cf (&a)[8], LAS cf* buf  , int lane) {
  const int k1 = lane >> 3, n3 = lane & 7;
  dft8<INV>(a);
  {
    cf w[8]; twid7(w, (float)lane * (1.0f / 512.0f));
#pragma unroll
    for (int k = 1; k < 8; ++k) a[k] = ctw<INV>(a[k], w[k]);
  }
#pragma unroll
  for (int k = 0; k < 8; ++k) buf[k * 72 + lane] = a[k];
  wave_sync();
#pragma unroll
  for (int j = 0; j < 8; ++j) a[j] = buf[k1 * 72 + n3 + 8 * j];
  dft8<INV>(a);
  {
    cf w[8]; twid7(w, (float)n3 * (1.0f / 64.0f));
#pragma unroll
    for (int k = 1; k < 8; ++k) a[k] = ctw<INV>(a[k], w[k]);
  }
  wave_sync();
#pragma unroll
  for (int k = 0; k < 8; ++k) buf[k1 * 72 + 9 * k + n3] = a[k];
  wave_sync();
#pragma unroll
  for (int j = 0; j < 8; ++j) a[j] = buf[k1 * 72 + 9 * n3 + j];
  dft8<INV>(a);
}

constexpr unsigned L_X = 0  , L_W = 66560  , L_K = 0, L_V = 65536,
                   L_ONES = 132096, L_ZERO = L_ONES + 2048, L_FFT = L_ZERO + 2048, L_QS = L_FFT + 18688  ,
                   L_SYNC = L_QS + 8192, LDS_BYTES = L_SYNC + 256;
enum { SY_W0 = 0, SY_W1, SY_W2, SY_W3, SY_X, SY_KVPUB, SY_GO1, SY_XWFREE, SY_QS, SY_F1A, SY_F1B, SY_PUB2, SY_GO2, SY_KV0, SY_KV1, SY_FB, SY_PUB3, SY_GO3,
       SY_FFTDONE, SY_PUB4, SY_GO4, SY_PARKED, SY_PARK0, SY_PARK1, SY_PARK2, SY_PARK3, SY_ODONE, SY_OSH, SY_TAGOK, SY_TAG = 32   };
#define SY(i) (lb + L_SYNC + (i) * 4)
#define WDELAY lds_wait(SY(SY_X), 2)
constexpr unsigned SPIN_CAP = 1u << 16;

#define EX(i) Sc[i] = __builtin_amdgcn_exp2f(Sc[i]); SB()
#define CV(P, j, i) P[j] = pk_bf16(Sc[i], Sc[i + 1]); SB()
DI void att_step(f32x16& Sc, f32x16& Sn, f32x16& Op, f32x16& Oc, const f32x16& C, const bf16x8& qn, const bf16x8& kn, const bf16x8& v1p, const bf16x8& v0c,
                 u32x4& p1) {
  u32x4 p0;
  Sn = MFMA32(kn, qn, C); SB();
  EX(0); EX(1); EX(2); EX(3); CV(p0, 0, 0); EX(4); CV(p0, 1, 2); EX(5);
  Op = MFMA32(v1p, __builtin_bit_cast(bf16x8, p1), Op); SB();
  EX(6); EX(7); CV(p0, 2, 4); EX(8); CV(p0, 3, 6); EX(9); EX(10);
  Oc = MFMA32(v0c, __builtin_bit_cast(bf16x8, p0), Oc); SB();
  EX(11); CV(p1, 0, 8); EX(12); EX(13); CV(p1, 1, 10); EX(14); EX(15); CV(p1, 2, 12); CV(p1, 3, 14);
}
DI void poll_all(const char* fl, u32x2 tag, int lane) {
  for (unsigned spin = 0; spin < SPIN_CAP; ++spin) {
    u32x4 a, c;
    asm volatile("global_load_dwordx4 %0, %2, %3 sc1\n\tglobal_load_dwordx4 %1, %2, %3 offset:1024 sc1\n\ts_waitcnt vmcnt(0)"
                 : "=&v"(a), "=&v"(c) : "v"(lane * 16), "s"(fl) : "memory");
    const unsigned d = ((a[0] ^ tag[0]) | (a[2] ^ tag[0]) | (c[0] ^ tag[0]) | (c[2] ^ tag[0])) | ((a[1] ^ tag[1]) | (a[3] ^ tag[1]) | (c[1] ^ tag[1]) | (c[3] ^ tag[1]));
    if (wave_all(d == 0u)) break;
    __builtin_amdgcn_s_sleep(2);
  }
}

__attribute__((target("no-packed-fp32-ops"))) __global__ __launch_bounds__(768, 3)
void k_main(const u16* __restrict__ x, const u16* __restrict__ wq, const u16* __restrict__ wk, const u16* __restrict__ wv, const u16* __restrict__ wo,
            const u16* __restrict__ w_r, const u16* __restrict__ w_i, const u16* __restrict__ gw, char* __restrict__ ws, u16* __restrict__ out) {
  extern __shared__ __attribute__((aligned(16))) char lds_[];
  LAS char* lds = (LAS char*)lds_;
  const unsigned lb = (unsigned)(uintptr_t)lds;
  const int tid = threadIdx.x, wid = __builtin_amdgcn_readfirstlane(tid >> 6), lane = tid & 63, b = blockIdx.x;
  const int xg = b & 7, t0 = xg * 32 + (b >> 3), rm = b >> 6, nb = (b >> 3) & 7;
  const int rb0 = xg + 16 * rm;
  const int r = lane & 31, h = lane >> 5;
  TDECL;
  TS(0);
  if (tid < 64) *(LAS unsigned*)(lds + L_SYNC + tid * 4) = 0u;
  else if (tid >= 256) { const unsigned c = tid < 512 ? 0x3f803f80u : 0u; *(LAS u32x2*)(lds + L_ONES + (tid - 256) * 8) = u32x2{c, c}; }
  asm volatile("s_waitcnt lgkmcnt(0)" ::: "memory");
  __builtin_amdgcn_s_barrier();
  MEMBAR();

  if (wid < 8) {
    const int pr = wid & 3, wr = pr >> 1, hp = pr & 1, kh = wid >> 2;
    bf16x8 qA, qB;
    if (kh == 0) {
      {
        unsigned vw[4];
#pragma unroll
        for (int i = 0; i < 4; ++i) vw[i] = (unsigned)((7 * i + (lane >> 3)) * 1024 + (((lane & 7) ^ (((lane >> 4) & 1) << 2)) << 4));
        const char* wb = (const char*)wq + (size_t)wid * 32768 + nb * 128;
        WDELAY;
#pragma unroll
        for (int c = 0; c < 4; ++c) dma16x4(wb + c * 131072, vw[0], vw[1], vw[2], vw[3], lb + L_W + c * 16384 + wid * 4096);
      }
      const int g = lane >> 4, q = (lane & 15) >> 2, p = lane & 3;
      LAS char* wbase = lds + L_W + (8 * h + q) * 128 + ((hp ^ (q >> 1)) * 64) + 32 * (g & 1) + 8 * p;
      LAS char* xbase = lds + L_X + (32 * wr + r) * 1040 + h * 16;
      f32x16 acc = {};
      wait_vm<8>();  lds_signal(SY(SY_W0), lane);
      wait_vm<0>();  lds_signal(SY(SY_W1), lane);
      lds_wait(SY(SY_X), 4);
      bf16x8 xf[2][4]; s16x4 lo[2][4], hi[2][4];
      auto fetch = [&](int hc, int s_) __attribute__((always_inline)) {
        if ((hc & 3) == 0) lds_wait(SY(SY_W0 + (hc >> 2)), 4);
#pragma unroll
        for (int t = 0; t < 4; ++t) {
          xf[s_][t] = *(LAS bf16x8*)(xbase + (hc * 4 + t) * 32);
          lo[s_][t] = __builtin_amdgcn_ds_read_tr16_b64_v4i16((LAS s16x4*)(wbase + (hc * 4 + t) * 2048));
          hi[s_][t] = __builtin_amdgcn_ds_read_tr16_b64_v4i16((LAS s16x4*)(wbase + (hc * 4 + t) * 2048 + 512));
        }
        SB();
      };
      TS(1);
      fetch(0, 0);
#pragma unroll
      for (int hc = 0; hc < 8; ++hc) {
        if (hc < 7) fetch(hc + 1, (hc + 1) & 1);
#pragma unroll
        for (int t = 0; t < 4; ++t) acc = MFMA32(__builtin_shufflevector(lo[hc & 1][t], hi[hc & 1][t], 0, 1, 2, 3, 4, 5, 6, 7), xf[hc & 1][t], acc);
        SB();
      }
      lds_signal(SY(SY_XWFREE), lane);
      constexpr float C = 0.25f * 1.4426950408889634f;
      u32x4 oa, ob;
#pragma unroll
      for (int i = 0; i < 4; ++i) { oa[i] = pk_bf16(acc[2 * i] * C, acc[2 * i + 1] * C); ob[i] = pk_bf16(acc[8 + 2 * i] * C, acc[9 + 2 * i] * C); }
      qA = __builtin_bit_cast(bf16x8, oa); qB = __builtin_bit_cast(bf16x8, ob);
      lds_wait(SY(SY_KVPUB), 4);
      *(LAS u32x4*)(lds + L_QS + pr * 2048 + lane * 16) = oa;
      *(LAS u32x4*)(lds + L_QS + pr * 2048 + 1024 + lane * 16) = ob;
      lds_signal(SY(SY_QS), lane);
      TS(2);
    } else {
      u32x2 ep;
      __builtin_amdgcn_s_setprio(3);
      {
        const unsigned v0 = 64 + lane * 16;
        const char* xb = (const char*)x + (size_t)((rb0 + 8 * wr) * 32 + hp * 16) * 1024 - 64;
#pragma unroll
        for (int gi = 0; gi < 4; ++gi) dma16x4p(xb + gi * 4096, v0, v0 - 16, v0 - 32, v0 - 48, lb + L_X + (pr * 16 + gi * 4) * 1040);
        u32x2 kv0, kv1;
        const u16* kvs = (nb < 4 ? wk : wv) + 4 * (nb & 3) + pr * 2048;
        asm volatile("global_load_dwordx2 %0, %3, %4\n\tglobal_load_dwordx2 %1, %3, %4 offset:2048\n\tglobal_load_dwordx2 %2, %5, %6 sc1\n\ts_waitcnt vmcnt(0)"
                     : "=&v"(kv0), "=&v"(kv1), "=&v"(ep) : "v"(lane * 32), "s"(kvs), "v"(0), "s"(ws + WS_EP) : "memory");
        *(LAS u32x2*)(lds + L_QS + pr * 1024 + lane * 8) = kv0;
        *(LAS u32x2*)(lds + L_QS + pr * 1024 + 512 + lane * 8) = kv1;
        lds_signal(SY(SY_X), lane);
      }
      const u32x4 gq = *(const u32x4*)(gw + lane * 8);
      lds_wait(SY(SY_X), 4);
      TS(1);
      {
        const int g = lane >> 4, q = (lane & 15) >> 2, row2 = 16 * pr + (lane & 15);
        LAS char* ab = lds + L_QS + (8 * g + q) * 8;
        LAS char* xb2 = lds + L_X + row2 * 1040 + g * 16;
        f32x4 acc2 = {};
        const int tile = rb0 + 8 * wr;
        bf16x8 yf[16], af[16];
#pragma unroll
        for (int t = 0; t < 16; ++t) {
          yf[t] = *(LAS bf16x8*)(xb2 + t * 64);
          const s16x4 l2 = __builtin_amdgcn_ds_read_tr16_b64_v4i16((LAS s16x4*)(ab + t * 256)), h2 = __builtin_amdgcn_ds_read_tr16_b64_v4i16((LAS s16x4*)(ab + t * 256 + 32));
          af[t] = __builtin_shufflevector(l2, h2, 0, 1, 2, 3, 4, 5, 6, 7);
        }
        SB();
        if (nb < 4) {
#pragma unroll
          for (int t = 0; t < 16; ++t) acc2 = MFMA16(af[t], yf[t], acc2);
          const unsigned p0 = pk_bf16(acc2[0], acc2[1]), p1 = pk_bf16(acc2[2], acc2[3]);
          if (lane < 16) st8_dev(ws + WS_K + tile * 1024 + (nb & 1) * 512 + (row2 & 31) * 16 + (nb >> 1) * 8, u32x2{p0, p1});
          float n2 = bf_lo(p0) * bf_lo(p0) + bf_hi(p0) * bf_hi(p0) + bf_lo(p1) * bf_lo(p1) + bf_hi(p1) * bf_hi(p1);
          n2 = row16_max(n2);
          if (lane == 0) st4_dev(ws + WS_KP + ((tile * 2 + hp) * 4 + nb) * 4, __builtin_bit_cast(unsigned, n2));
        } else {
#pragma unroll
          for (int t = 0; t < 16; ++t) acc2 = MFMA16(yf[t], af[t], acc2);
          if ((lane & 15) < 4)
            st8_dev(ws + WS_V + tile * 1024 + hp * 512 + (g & 1) * 256 + ((nb - 4) * 4 + (lane & 15)) * 16 + (g >> 1) * 8, u32x2{pk_bf16(acc2[0], acc2[1]), pk_bf16(acc2[2], acc2[3])});
        }
      }
      const int r0 = 8 * nb + 2 * pr;
      const u32x4 xa = *(LAS u32x4*)(lds + L_X + r0 * 1040 + lane * 16);
      const u32x4 xb = *(LAS u32x4*)(lds + L_X + (r0 + 1) * 1040 + lane * 16);
      wait_vm<0>();
      lds_signal(SY(SY_XWFREE), lane);
      const unsigned tk = lds_ticket(SY(SY_KVPUB), lane);
      TS(2);
      u32x2 tag;
      { const unsigned long long e = (((unsigned long long)ep[1] << 32) | ep[0]) + 1ull; tag = u32x2{(unsigned)e, (unsigned)(e >> 32)}; }
      if (tk == 3) {
        if (lane < 8) st8_dev(ws + WS_FL + lane * 2048 + b * 8, tag);
        if (lane == 0) *(LAS u32x2*)(lds + L_SYNC + SY_TAG * 4) = tag;
        lds_signal(SY(SY_TAGOK), lane);
      }
      {
        float gs1 = 0.f, gs2 = 0.f;
#pragma unroll
        for (int i = 0; i < 4; ++i) {
          const unsigned pa = pk_bf16(bf_lo(xa[i]) * bf_lo(gq[i]), bf_hi(xa[i]) * bf_hi(gq[i])), pb = pk_bf16(bf_lo(xb[i]) * bf_lo(gq[i]), bf_hi(xb[i]) * bf_hi(gq[i]));
          gs1 += bf_lo(pa) + bf_hi(pa); gs2 += bf_lo(pb) + bf_hi(pb);
        }
        gs1 = wave_sum(gs1); gs2 = wave_sum(gs2);
        if (lane < 2) {
          const float z = bfr(lane ? gs2 : gs1);
          st4_dev(ws + WS_G + ((rb0 + 8 * (r0 >> 5)) * 32 + (r0 & 31) + lane) * 4,
                  __builtin_bit_cast(unsigned, bfr(__builtin_amdgcn_rcpf(1.0f + __builtin_amdgcn_exp2f(z * -1.4426950408889634f)))));
        }
      }
      wait_vm<0>();
      lds_signal(SY(SY_PUB2), lane);
      if (tk == 3) {
        poll_all(ws + WS_FL + xg * 2048, tag, lane);
        lds_signal(SY(SY_GO1), lane);
        if (b == 0 && lane == 0) st8_dev(ws + WS_EP, tag);
      }
      __builtin_amdgcn_s_setprio(0);
      lds_wait(SY(SY_QS), 4);
      qA = *(LAS bf16x8*)(lds + L_QS + pr * 2048 + lane * 16);
      qB = *(LAS bf16x8*)(lds + L_QS + pr * 2048 + 1024 + lane * 16);
    }
    float qn2;
    {
      const u32x4 qu = __builtin_bit_cast(u32x4, qA), qv = __builtin_bit_cast(u32x4, qB);
      float na = 0.f, nq = 0.f;
#pragma unroll
      for (int i = 0; i < 4; ++i) { float a = bf_lo(qu[i]), c = bf_hi(qu[i]); na += a * a + c * c; a = bf_lo(qv[i]); c = bf_hi(qv[i]); nq += a * a + c * c; }
      qn2 = fmaxf(x32_sum(na), x32_sum(nq));
    }
    lds_wait(SY(SY_XWFREE), 8);
    lds_wait(SY(SY_GO1), 1);
    TS(3);
    f32x4 kp0, kp1;
    asm volatile("global_load_dwordx4 %0, %2, %3 sc1\n\tglobal_load_dwordx4 %1, %2, %3 offset:1024 sc1" : "=&v"(kp0), "=&v"(kp1) : "v"(lane * 16), "s"(ws + WS_KP) : "memory");
    { const int off = (pr * 32 + kh * 4) * 1024; dma16x4_dev(ws + WS_K + off, lane * 16, lb + off); }
    asm volatile("s_waitcnt vmcnt(4)" : "+v"(kp0), "+v"(kp1) :: "memory");
    const float km = wave_max(fmaxf(kp0[0] + kp0[1] + kp0[2] + kp0[3], kp1[0] + kp1[1] + kp1[2] + kp1[3]));
    const float negm = -__builtin_amdgcn_sqrtf(qn2 * km) * 1.0001f;
    LAS char* kp = lds + L_K + lane * 16;
    const int vstep = (r < 16) ? 2048 : 0;
    const bool onesrow = (r & 27) == 16;
    f32x16 OA = {}, OB = {};
    {
      f32x16 C;
#pragma unroll
      for (int i = 0; i < 16; ++i) C[i] = negm;
      int va = (r < 16) ? (L_V + kh * 32768 + h * 256 + r * 16) : ((onesrow ? L_ONES : L_ZERO) + h * 256 + (r - 16) * 16);
      int ka = kh * 32768;
      wait_vm<0>();
      lds_signal(SY(SY_KV0), lane);
      lds_wait(SY(SY_KV0), 8);
      TS(4);
#define LDK(o) (*(LAS bf16x8*)(kp + ka + (o)))
#define LDV(o) (*(LAS bf16x8*)(lds + va + (o)))
      bf16x8 k0 = LDK(0), k1, v00 = LDV(0), v10 = LDV(512), v01, v11 = v10;
      f32x16 Sa = MFMA32(k0, qA, C), Sb;
      u32x4 p1 = {0u, 0u, 0u, 0u};
#pragma unroll 1
      for (int ph = 0; ph < 2; ++ph) {
        const int nit = ph ? 13 : 3;
#pragma unroll 1
        for (int it = 0; it < nit; ++it) {
          k1 = LDK(1024); v01 = LDV(1024); SB();
          att_step(Sa, Sb, OB, OA, C, qB, k0, v11, v00, p1);
          v11 = LDV(1024 + 512); SB();
          att_step(Sb, Sa, OA, OB, C, qA, k1, v10, v00, p1);
          va += vstep;
          k0 = LDK(2048); v00 = LDV(0); SB();
          att_step(Sa, Sb, OB, OA, C, qB, k1, v10, v01, p1);
          v10 = LDV(512); SB();
          att_step(Sb, Sa, OA, OB, C, qA, k0, v11, v01, p1);
          ka += 2048;
        }
        if (ph == 0) lds_wait(SY(SY_KV1), 4);
      }
      OB = MFMA32(v11, __builtin_bit_cast(bf16x8, p1), OB);
    }
    TS(5);
    LAS float* red = (LAS float*)(lds + L_FFT + pr * 4608) + lane;
    const int rbw = rb0 + 8 * wr;
    if (kh == 0) {
      lds_wait_slow(SY(SY_FFTDONE), 4);
#pragma unroll
      for (int i = 0; i < 9; ++i) { red[i * 64] = OA[i]; red[(9 + i) * 64] = OB[i]; }
      lds_signal(SY(SY_PARK0 + pr), lane);
      lds_signal(SY(SY_PARKED), lane);
      lds_wait_slow(SY(SY_PARKED), 4);
      lds_wait_slow(SY(SY_ODONE), 1);
      const char* og = ws + WS_O + (size_t)rbw * 32768;
      const unsigned dst = (wr ? L_V : L_K) + hp * 14 * 1024 + lane * 16;
      {
        u32x4 f[14];
        unsigned o[7];
#pragma unroll
        for (int i = 0; i < 7; ++i) o[i] = (unsigned)(((nb * 4 + 4 + hp * 14 + 2 * i) & 31) * 1024 + lane * 16);
        for (unsigned spin = 0; spin < SPIN_CAP; ++spin) {
#define LD2(i) asm volatile("global_load_dwordx4 %0, %2, %3 sc1\n\tglobal_load_dwordx4 %1, %2, %3 offset:1024 sc1" : "=&v"(f[2 * i]), "=&v"(f[2 * i + 1]) : "v"(o[i]), "s"(og) : "memory")
          LD2(0); LD2(1); LD2(2); LD2(3); LD2(4); LD2(5); LD2(6);
          asm volatile("s_waitcnt vmcnt(0)" : "+v"(f[0]), "+v"(f[1]), "+v"(f[2]), "+v"(f[3]), "+v"(f[4]), "+v"(f[5]), "+v"(f[6]), "+v"(f[7]), "+v"(f[8]), "+v"(f[9]),
                       "+v"(f[10]), "+v"(f[11]), "+v"(f[12]), "+v"(f[13]) :: "memory");
          unsigned m = 0u;
#pragma unroll
          for (int i = 0; i < 14; ++i) m = umax(m, umax(umax(f[i][0], f[i][1]), umax(f[i][2], f[i][3])));
          if (wave_all(m != SENT)) break;
        }
#pragma unroll
        for (int i = 0; i < 14; ++i) *(LAS u32x4*)(lds + dst + i * 1024) = f[i];
      }
      lds_signal(SY(SY_OSH), lane);
      TS(6);
      TDUMP(0);
      return;
    }
    lds_wait(SY(SY_PARK0 + pr), 1);
#pragma unroll
    for (int i = 0; i < 9; ++i) { OA[i] += red[i * 64]; OB[i] += red[(9 + i) * 64]; }
    if (!wave_all(OA[8] >= 1.2621774483536189e-29f && OB[8] >= 1.2621774483536189e-29f)) {
      __builtin_amdgcn_fence(__ATOMIC_ACQUIRE, "agent");
      const bf16x8* kg = (const bf16x8*)(ws + WS_K) + lane;
      const bf16x8* vg = (const bf16x8*)(ws + WS_V + h * 256 + (r & 15) * 16);
      const short cv = onesrow ? (short)0x3f80 : (short)0;
      const bf16x8 vc = {cv, cv, cv, cv, cv, cv, cv, cv};
#pragma unroll 1
      for (int hh = 0; hh < 2; ++hh) {
        const bf16x8 qf = hh ? qB : qA;
        float m = -__builtin_inff();
        f32x16 C = {};
#pragma unroll 1
        for (int t = 0; t < 64; ++t) {
          f32x16 Sx = MFMA32(kg[t * 64], qf, C);
#pragma unroll
          for (int i = 0; i < 16; ++i) m = fmaxf(m, Sx[i]);
        }
        m = x32_max(m);
#pragma unroll
        for (int i = 0; i < 16; ++i) C[i] = -m;
        f32x16 O = {};
#pragma unroll 1
        for (int t = 0; t < 64; ++t) {
          f32x16 Sx = MFMA32(kg[t * 64], qf, C);
          const bf16x8 va0 = vg[t * 64], va1 = vg[t * 64 + 32];
          u32x4 p0, p1;
#pragma unroll
          for (int i = 0; i < 4; ++i) {
            p0[i] = pk_bf16(__builtin_amdgcn_exp2f(Sx[2 * i]), __builtin_amdgcn_exp2f(Sx[2 * i + 1]));
            p1[i] = pk_bf16(__builtin_amdgcn_exp2f(Sx[8 + 2 * i]), __builtin_amdgcn_exp2f(Sx[9 + 2 * i]));
          }
          O = MFMA32(r < 16 ? va0 : vc, __builtin_bit_cast(bf16x8, p0), O);
          O = MFMA32(r < 16 ? va1 : vc, __builtin_bit_cast(bf16x8, p1), O);
        }
        if (hh) OB = O; else OA = O;
      }
    }
    {
      const float ia = __builtin_amdgcn_rcpf(OA[8]), ib = __builtin_amdgcn_rcpf(OB[8]);
      u32x4 fa, fb;
#pragma unroll
      for (int i = 0; i < 4; ++i) {
        fa[i] = pk_bf16(OA[2 * i] * ia, OA[2 * i + 1] * ia); fb[i] = pk_bf16(OB[2 * i] * ib, OB[2 * i + 1] * ib);
        if (fa[i] == SENT) fa[i] = 0x7fc07fc0u;
        if (fb[i] == SENT) fb[i] = 0x7fc07fc0u;
      }
      char* og = ws + WS_O + ((size_t)(rbw * 32 + nb * 4 + hp * 2) * 64 + lane) * 16;
      st16_dev(og, fa); st16_dev(og + 1024, fb);
      wave_sync();
      *(LAS u32x4*)(lds + L_FFT + pr * 4608 + lane * 16) = fa;
      *(LAS u32x4*)(lds + L_FFT + pr * 4608 + 1024 + lane * 16) = fb;
      lds_signal(SY(SY_ODONE), lane);
    }
    TS(6);
    TDUMP(1);
    return;
  }

  __builtin_amdgcn_s_setprio(2);
  const int w = wid - 8, t = tid - 512;
  LAS cf* buf = (LAS cf*)(lds + L_FFT);
  LAS cf* wbuf = buf + w * 576;
  {
    const int sA = t0 + 512 * w;
    const u32x4 xa = *(const u32x4*)(x + (size_t)sA * D + lane * 8), xb = *(const u32x4*)(x + (size_t)(sA + 256) * D + lane * 8);
    *(LAS u32x4*)((LAS char*)wbuf + lane * 16) = xa;
    *(LAS u32x4*)((LAS char*)wbuf + 1024 + lane * 16) = xb;
    wave_sync();
    cf a[8];
#pragma unroll
    for (int j = 0; j < 8; ++j) a[j] = cf{bf_f(((LAS u16*)wbuf)[lane + 64 * j]), bf_f(((LAS u16*)wbuf)[512 + lane + 64 * j])};
    wave_sync();
    fft512_wave<0>(a, wbuf, lane);
    const int ub = (t0 >> 5) + 8 * ((t0 >> 2) & 7), vb = (t0 & 3) * 64 + w * 16;
    typedef unsigned u32x4_u __attribute__((ext_vector_type(4), aligned(2)));
    const size_t wofs = (size_t)(ub + 64 * (lane >> 1)) * DF + vb + (lane & 1) * 8;
    const u32x4 fr = *(const u32x4_u*)(w_r + wofs), fi = *(const u32x4_u*)(w_i + wofs);
    u16 wov[8];
    const int frag = 2 * t0 + (w & 1);
    if (w < 2) {
#pragma unroll
      for (int j = 0; j < 8; ++j) wov[j] = wo[(16 * (frag & 31) + 8 * (j >> 2) + 4 * h + (j & 3)) * D + 32 * (frag >> 5) + r];
    } else if (t < 136) {
      const int j = t - 128, u = ub + 128 * (t0 & 3) + 64 * (j >> 2) + 512 * (j & 3), un = (S - u) & (S - 1);
      wov[0] = w_r[u * DF]; wov[1] = w_i[u * DF]; wov[2] = w_r[un * DF]; wov[3] = w_i[un * DF];
      wov[4] = w_r[u * DF + 256]; wov[5] = w_i[u * DF + 256]; wov[6] = w_r[un * DF + 256]; wov[7] = w_i[un * DF + 256];
    }
    const int kl = (lane >> 3) + 8 * (lane & 7);
    wave_sync();
#pragma unroll
    for (int k = 0; k < 8; ++k) wbuf[k * 64 + kl] = a[k];
    lds_signal(SY(SY_F1A), lane);
    cf tw[8]; twid7(tw, (float)t0 * (1.0f / 2048.0f));
    constexpr float SC = 1.0f / 2097152.0f;
    lds_wait(SY(SY_F1A), 4);
    {
      const int mi = t ? 512 - t : 256;
#pragma unroll
      for (int q = 0; q < 4; ++q) {
        const cf A = buf[q * 576 + t] * SC, B = buf[q * 576 + mi] * SC;
        a[2 * q]     = t ? cf{A.x + B.x, A.y - B.y} : cf{2.f * A.x, 2.f * B.x};
        a[2 * q + 1] = t ? cf{A.y + B.y, B.x - A.x} : cf{2.f * A.y, 2.f * B.y};
      }
    }
    lds_signal(SY(SY_F1B), lane);
    dft8<0>(a);
#pragma unroll
    for (int k = 1; k < 8; ++k) a[k] = cmul(a[k], tw[k]);
    lds_wait(SY(SY_F1B), 4);
#pragma unroll
    for (int q = 0; q < 4; ++q) ((LAS f32x4*)wbuf)[lane * 4 + (q ^ ((lane >> 1) & 3))] = f32x4{a[2 * q].x, a[2 * q].y, a[2 * q + 1].x, a[2 * q + 1].y};
    wave_sync();
    {
      char* xo = ws + WS_X1 + (size_t)t0 * 16384 + w * 4096 + lane * 16;
#pragma unroll
      for (int m = 0; m < 4; ++m) { const int p = m * 64 + lane; st16_dev(xo + m * 1024, ((LAS u32x4*)wbuf)[p ^ ((p >> 3) & 3)]); }
    }
    TS(1);
    wave_sync();
    {
      LAS char* st = (LAS char*)wbuf;
      LAS unsigned* d0 = (LAS unsigned*)(st + (lane >> 1) * 36 + (lane & 1) * 16);
#pragma unroll
      for (int i = 0; i < 4; ++i) { d0[i] = fr[i]; d0[288 + i] = fi[i]; }
      wave_sync();
      const int q = lane & 7;
#pragma unroll
      for (int n = 0; n < 2; ++n) {
        const int vv = (lane >> 3) + 8 * n;
        const LAS u16* sr = (const LAS u16*)(st + q * 36) + vv;
        u32x4 o;
#pragma unroll
        for (int i = 0; i < 4; ++i) o[i] = (unsigned)sr[i * 144] | ((unsigned)sr[576 + i * 144] << 16);
        st16_dev(ws + WS_WT + (size_t)(vb + vv) * WT_STRIDE + (t0 >> 2) * 128 + q * 16, o);
      }
    }
    if (w < 2) {
      u32x4 o;
#pragma unroll
      for (int j = 0; j < 4; ++j) o[j] = (unsigned)wov[2 * j] | ((unsigned)wov[2 * j + 1] << 16);
      st16_dev(ws + WS_WO + (size_t)frag * 1024 + lane * 16, o);
    } else if (t < 136) {
      const cf h0 = {bf_f(wov[0]) + bf_f(wov[2]), bf_f(wov[1]) - bf_f(wov[3])}, h1 = {bf_f(wov[4]) + bf_f(wov[6]), bf_f(wov[5]) - bf_f(wov[7])};
      const cf P = (h0 + h1) * 0.25f, M = (h0 - h1) * 0.25f;
      st16_dev(ws + WS_PM + (t0 * 8 + (t - 128)) * 16, __builtin_bit_cast(u32x4, f32x4{P.x, P.y, M.x, M.y}));
    }
    {
      char* of = ws + WS_O + ((size_t)(rb0 + 8 * (w >> 1)) * 32 + nb * 4) * 1024 + (t & 127) * 16;
      st16_dev(of, u32x4{SENT, SENT, SENT, SENT}); st16_dev(of + 2048, u32x4{SENT, SENT, SENT, SENT});
    }
    wait_vm<0>();
    lds_signal(SY(SY_PUB2), lane);
  }
  TS(2);
  lds_wait(SY(SY_KVPUB), 4);
  lds_wait(SY(SY_TAGOK), 1);
  const u32x2 tag = *(LAS u32x2*)(lds + L_SYNC + SY_TAG * 4);
  if (w == 0) {
    lds_wait(SY(SY_PUB2), 8);
    if (lane < 8) st8_dev(ws + WS_FL + 16384 + lane * 2048 + b * 8, tag);
  }
  lds_wait(SY(SY_KV0), 8);
#pragma unroll
  for (int i = 0; i < 6; ++i) { const int off = (w * 32 + 8 + 4 * i) * 1024; dma16x4_dev(ws + WS_K + off, lane * 16, lb + off); }
  wait_vm<0>();
  lds_signal(SY(SY_KV1), lane);
  if (w == 0) { poll_all(ws + WS_FL + 16384 + xg * 2048, tag, lane); lds_signal(SY(SY_GO2), lane); }
  unsigned fbn = 0;
#define FFT_BAR() do { fbn += 4; lds_signal(SY(SY_FB), lane); lds_wait(SY(SY_FB), fbn); } while (0)
  {
    const int v = t0;
    const int k1n = lane & 7, in_ = (lane >> 3) + 8 * w;
    const int k1 = t >> 5, k2 = (t >> 2) & 7, ip = t & 3;
    cf twA[8], twB[8], twC[8];
    twid7(twB, (float)in_ * (1.0f / 256.0f));
    twid7(twC, (float)ip * (1.0f / 32.0f));
    twid7(twA, (float)t * (1.0f / 2048.0f));
    LAS cf* bN = buf + k1n * 292 + in_;
    LAS cf* bO = buf + k1 * 292 + k2 * 36 + ip;
    LAS cf* bP = buf + k1 * 292 + k2 * 36 + ip * 8;
    LAS cf* bA = buf + (t >> 5) * 36 + (t & 31);
    lds_wait(SY(SY_GO2), 1);
    TS(3);
    cf a[8]; u32x4 wv0, wv1;
    {
      const unsigned xo = (unsigned)((in_ * 256 + v) * 64 + k1n * 8);
      const char* xb = ws + WS_X1;
#define LDX(j) asm volatile("global_load_dwordx2 %0, %1, %2 sc1" : "=&v"(a[j]) : "v"(xo), "s"(xb + (size_t)(j) * 524288) : "memory")
      LDX(0); LDX(4); LDX(1); LDX(5); LDX(2); LDX(6); LDX(3); LDX(7);
      const char* wt = ws + WS_WT + (size_t)v * WT_STRIDE;
      asm volatile("global_load_dwordx4 %0, %2, %3 sc1\n\tglobal_load_dwordx4 %1, %2, %3 offset:16 sc1" : "=&v"(wv0), "=&v"(wv1) : "v"(t * 32), "s"(wt) : "memory");
    }
    asm volatile("s_waitcnt vmcnt(0)" : "+v"(a[0]), "+v"(a[1]), "+v"(a[2]), "+v"(a[3]), "+v"(a[4]), "+v"(a[5]), "+v"(a[6]), "+v"(a[7]), "+v"(wv0), "+v"(wv1) :: "memory");
    dft8<0>(a);
#pragma unroll
    for (int k = 1; k < 8; ++k) a[k] = cmul(a[k], twB[k]);
#pragma unroll
    for (int k = 0; k < 8; ++k) bN[36 * k] = a[k];
    FFT_BAR();
#pragma unroll
    for (int j = 0; j < 8; ++j) a[j] = bO[4 * j];
    dft8<0>(a);
#pragma unroll
    for (int k = 1; k < 8; ++k) a[k] = cmul(a[k], twC[k]);
#pragma unroll
    for (int k = 0; k < 8; ++k) bO[4 * k] = a[k];
    wave_sync();
#pragma unroll
    for (int j = 0; j < 8; ++j) a[j] = bP[j];
    dft4<0>(a[0], a[1], a[2], a[3]);
    dft4<0>(a[4], a[5], a[6], a[7]);
    if (v != 0) {
#pragma unroll
      for (int j = 0; j < 4; ++j) { a[j] = cmul(a[j], cf{bf_lo(wv0[j]), bf_hi(wv0[j])}); a[4 + j] = cmul(a[4 + j], cf{bf_lo(wv1[j]), bf_hi(wv1[j])}); }
    } else {
      f32x4 pm[8];
#define LDP(j) asm volatile("global_load_dwordx4 %0, %1, %2 offset:" #j "*16 sc1" : "=&v"(pm[j]) : "v"(t * 128), "s"(ws + WS_PM) : "memory")
      LDP(0); LDP(1); LDP(2); LDP(3); LDP(4); LDP(5); LDP(6); LDP(7);
      FFT_BAR();
#pragma unroll
      for (int j = 0; j < 8; ++j) buf[k1 + 8 * k2 + 64 * (2 * ip + (j >> 2)) + 512 * (j & 3)] = a[j];
      FFT_BAR();
      asm volatile("s_waitcnt vmcnt(0)" : "+v"(pm[0]), "+v"(pm[1]), "+v"(pm[2]), "+v"(pm[3]), "+v"(pm[4]), "+v"(pm[5]), "+v"(pm[6]), "+v"(pm[7]) :: "memory");
#pragma unroll
      for (int j = 0; j < 8; ++j) {
        const int u = k1 + 8 * k2 + 64 * (2 * ip + (j >> 2)) + 512 * (j & 3);
        cf cn = buf[(S - u) & (S - 1)]; cn.y = -cn.y;
        a[j] = cmul(a[j], cf{pm[j].x, pm[j].y}) + cmul(cn, cf{pm[j].z, pm[j].w});
      }
      FFT_BAR();
    }
    dft4<1>(a[0], a[1], a[2], a[3]);
    dft4<1>(a[4], a[5], a[6], a[7]);
#pragma unroll
    for (int j = 0; j < 8; ++j) bP[j] = a[j];
    wave_sync();
#pragma unroll
    for (int k = 0; k < 8; ++k) a[k] = bO[4 * k];
#pragma unroll
    for (int k = 1; k < 8; ++k) a[k] = cmulc(a[k], twC[k]);
    dft8<1>(a);
#pragma unroll
    for (int j = 0; j < 8; ++j) bO[4 * j] = a[j];
    FFT_BAR();
#pragma unroll
    for (int k = 0; k < 8; ++k) a[k] = bN[36 * k];
#pragma unroll
    for (int k = 1; k < 8; ++k) a[k] = cmulc(a[k], twB[k]);
    dft8<1>(a);
#pragma unroll
    for (int j = 0; j < 8; ++j) bN[36 * j] = a[j];
    FFT_BAR();
#pragma unroll
    for (int k = 0; k < 8; ++k) a[k] = bA[292 * k];
#pragma unroll
    for (int k = 1; k < 8; ++k) a[k] = cmulc(a[k], twA[k]);
    dft8<1>(a);
#pragma unroll
    for (int j = 0; j < 8; ++j) bA[292 * j] = a[j];
    wave_sync();
    {
      const int te = t & ~1, jo = (t & 1) * 4;
      char* zb = ws + WS_Z + (size_t)((te >> 3) + jo * 32) * Z_STRIDE + v * 64 + (te & 7) * 8;
      const LAS cf* src = bA - (t & 1) + 292 * jo;
#pragma unroll
      for (int j = 0; j < 4; ++j) st16_dev(zb + (size_t)j * 32 * Z_STRIDE, *(const LAS u32x4*)(src + 292 * j));
    }
    wait_vm<0>();
    TS(4);
    if (lds_ticket(SY(SY_PUB3), lane) == 3) {
      if (lane < 8) st8_dev(ws + WS_FL + 32768 + lane * 2048 + b * 8, tag);
      poll_all(ws + WS_FL + 32768 + xg * 2048, tag, lane);
      lds_signal(SY(SY_GO3), lane);
    }
    lds_wait_slow(SY(SY_GO3), 1);
  }
  TS(5);
  {
    const int rbf = rb0 + 8 * (nb >> 2), sblk = rbf * 4 + (nb & 3);
    cf a[8];
    {
      const char* z = ws + WS_Z + (size_t)sblk * Z_STRIDE + w * 16;
      const unsigned o1 = lane * 64, o2 = ((64 - lane) & 63) * 64 + (lane ? 0 : 4096);
      f32x4 q[8];
      asm volatile("global_load_dwordx4 %0, %8, %10 sc1\n\tglobal_load_dwordx4 %1, %8, %11 sc1\n\tglobal_load_dwordx4 %2, %8, %12 sc1\n\tglobal_load_dwordx4 %3, %8, %13 sc1\n\t"
                   "global_load_dwordx4 %4, %9, %13 sc1\n\tglobal_load_dwordx4 %5, %9, %12 sc1\n\tglobal_load_dwordx4 %6, %9, %11 sc1\n\tglobal_load_dwordx4 %7, %9, %10 sc1\n\ts_waitcnt vmcnt(0)"
                   : "=&v"(q[0]), "=&v"(q[1]), "=&v"(q[2]), "=&v"(q[3]), "=&v"(q[4]), "=&v"(q[5]), "=&v"(q[6]), "=&v"(q[7])
                   : "v"(o1), "v"(o2), "s"(z), "s"(z + 4096), "s"(z + 8192), "s"(z + 12288) : "memory");
#pragma unroll
      for (int j = 0; j < 4; ++j) a[j] = cf{q[j].x - q[j].w, q[j].y + q[j].z};
#pragma unroll
      for (int j = 4; j < 8; ++j) a[j] = cf{q[j].x + q[j].w, q[j].z - q[j].y};
      if (lane == 0) { a[0] = cf{q[0].x, q[0].z}; a[4] = cf{q[0].y, q[0].w}; }
    }
    fft512_wave<1>(a, wbuf, lane);
    const int kl = (lane >> 3) + 8 * (lane & 7);
    wave_sync();
    LAS u16* rows = (LAS u16*)wbuf;
#pragma unroll
    for (int k = 0; k < 8; ++k) {
      const unsigned p = pk_bf16(a[k].x, a[k].y);
      rows[kl + 64 * k] = (u16)p; rows[520 + kl + 64 * k] = (u16)(p >> 16);
    }
    wave_sync();
    {
      const int par = lane >> 5, nt = (lane >> 1) & 15, hh = lane & 1;
      const LAS u16* src = rows + par * 520 + nt * 32 + hh * 4;
      u32x2 q[4];
#pragma unroll
      for (int g = 0; g < 4; ++g) q[g] = *(const LAS u32x2*)(src + 8 * g);
      char* bd = ws + WS_B + (((size_t)rbf * 16 + nt) * 64 + hh * 32 + (nb & 3) * 8 + w * 2 + par) * 32;
      st16_dev(bd, u32x4{q[0][0], q[0][1], q[1][0], q[1][1]});
      st16_dev(bd + 16, u32x4{q[2][0], q[2][1], q[3][0], q[3][1]});
    }
    lds_signal(SY(SY_FFTDONE), lane);
    wait_vm<0>();
    if (lds_ticket(SY(SY_PUB4), lane) == 3) {
      if (lane == 0) st8_dev(ws + WS_FL + 49152 + b * 8, tag);
      const unsigned fo = (unsigned)(((b & ~56) | ((lane & 7) << 3)) * 8);
      for (unsigned spin = 0; spin < SPIN_CAP; ++spin) {
        u32x2 f;
        asm volatile("global_load_dwordx2 %0, %1, %2 sc1\n\ts_waitcnt vmcnt(0)" : "=&v"(f) : "v"(fo), "s"(ws + WS_FL + 49152) : "memory");
        if (wave_all(f[0] == tag[0] && f[1] == tag[1])) break;
        __builtin_amdgcn_s_sleep(4);
      }
      lds_signal(SY(SY_GO4), lane);
    }
  }
  __builtin_amdgcn_s_setprio(0);
  {
    const int pwr = w >> 1, nt = nb * 2 + (w & 1), rbp = rb0 + 8 * pwr;
    bf16x8 wf[32];
    {
      const char* wof = ws + WS_WO + (size_t)nt * 32768;
#pragma unroll
      for (int k = 0; k < 32; k += 4) {
        const unsigned o = (unsigned)(((nb * 4 + k) & 31) * 1024 + lane * 16);
        asm volatile("global_load_dwordx4 %0, %4, %5 sc1\n\tglobal_load_dwordx4 %1, %4, %5 offset:1024 sc1\n\tglobal_load_dwordx4 %2, %4, %5 offset:2048 sc1\n\tglobal_load_dwordx4 %3, %4, %5 offset:3072 sc1"
                     : "=&v"(wf[k]), "=&v"(wf[k + 1]), "=&v"(wf[k + 2]), "=&v"(wf[k + 3]) : "v"(o), "s"(wof) : "memory");
      }
    }
    lds_wait_slow(SY(SY_GO4), 1);
    u32x4 b0, b1; float g;
    asm volatile("global_load_dwordx4 %0, %3, %4 sc1\n\tglobal_load_dwordx4 %1, %3, %4 offset:16 sc1\n\tglobal_load_dword %2, %5, %6 sc1\n\ts_waitcnt vmcnt(0)"
                 : "=&v"(b0), "=&v"(b1), "=&v"(g) : "v"(lane * 32), "s"(ws + WS_B + ((size_t)rbp * 16 + nt) * 2048), "v"(r * 4), "s"(ws + WS_G + rbp * 128) : "memory");
#pragma unroll
    for (int k = 0; k < 32; k += 4) asm volatile("" : "+v"(wf[k]), "+v"(wf[k + 1]), "+v"(wf[k + 2]), "+v"(wf[k + 3]));
    {
      const float omg = bfr(1.0f - g);
#pragma unroll
      for (int i = 0; i < 4; ++i) { b0[i] = pk_bf16(omg * bf_lo(b0[i]), omg * bf_hi(b0[i])); b1[i] = pk_bf16(omg * bf_lo(b1[i]), omg * bf_hi(b1[i])); }
    }
    TS(6);
    lds_wait_slow(SY(SY_ODONE), 2);
    lds_wait(SY(SY_ODONE), 4);
    __builtin_amdgcn_s_setprio(2);
    f32x16 acc = {};
#pragma unroll
    for (int k = 0; k < 4; ++k) acc = MFMA32(wf[k], *(LAS bf16x8*)(lds + L_FFT + (pwr * 2 + (k >> 1)) * 4608 + (k & 1) * 1024 + lane * 16), acc);
    lds_wait(SY(SY_OSH), 4);
    {
      LAS char* of = lds + (pwr ? L_V : L_K) + lane * 16;
#pragma unroll
      for (int k = 4; k < 32; ++k) acc = MFMA32(wf[k], *(LAS bf16x8*)(of + (k - 4) * 1024), acc);
    }
    u16* op = out + (size_t)(rbp * 32 + r) * D + nt * 32 + 4 * h;
#pragma unroll
    for (int q = 0; q < 4; ++q) {
      const unsigned t01 = q < 2 ? b0[2 * q] : b1[2 * q - 4], t23 = q < 2 ? b0[2 * q + 1] : b1[2 * q - 3];
      const unsigned a01 = pk_bf16(acc[4 * q], acc[4 * q + 1]), a23 = pk_bf16(acc[4 * q + 2], acc[4 * q + 3]);
      const unsigned g01 = pk_bf16(g * bf_lo(a01), g * bf_hi(a01)), g23 = pk_bf16(g * bf_lo(a23), g * bf_hi(a23));
      *(u32x2*)(op + 8 * q) = u32x2{pk_bf16(bf_lo(g01) + bf_lo(t01), bf_hi(g01) + bf_hi(t01)), pk_bf16(bf_lo(g23) + bf_lo(t23), bf_hi(g23) + bf_hi(t23))};
    }
    TS(7);
    TDUMP(2);
  }
}

extern "C" void kernel_launch(void* const* d_in, const int* in_sizes, int n_in, void* d_out, int out_size, void* d_ws, size_t ws_size,
                              hipStream_t stream) {
  (void)hipFuncSetAttribute((const void*)k_main, hipFuncAttributeMaxDynamicSharedMemorySize, LDS_BYTES);
  k_main<<<256, 768, LDS_BYTES, stream>>>((const u16*)d_in[0], (const u16*)d_in[1], (const u16*)d_in[2], (const u16*)d_in[3], (const u16*)d_in[4],
                                          (const u16*)d_in[6], (const u16*)d_in[7], (const u16*)d_in[8], (char*)d_ws, (u16*)d_out);
}
```

```cpp
#include <hip/hip_runtime.h>
#include <hip/hip_bf16.h>
#include <stdint.h>

typedef short    bf16x8 __attribute__((ext_vector_type(8)));
typedef short    s16x4  __attribute__((ext_vector_type(4)));
typedef float    f32x16 __attribute__((ext_vector_type(16)));
typedef float    f32x4  __attribute__((ext_vector_type(4)));
typedef float    f32x2  __attribute__((ext_vector_type(2)));
typedef unsigned u32x4  __attribute__((ext_vector_type(4)));
typedef unsigned u32x2  __attribute__((ext_vector_type(2)));
typedef __bf16   bf16x2_t __attribute__((ext_vector_type(2)));
typedef f32x2 cf;
typedef unsigned short u16;
#define DI __device__ __forceinline__
#define LAS __attribute__((address_space(3)))
#define GAS __attribute__((address_space(1)))
#define MFMA32(a, b, c) __builtin_amdgcn_mfma_f32_32x32x16_bf16((a), (b), (c), 0, 0, 0)
#define MFMA16(a, b, c) __builtin_amdgcn_mfma_f32_16x16x32_bf16((a), (b), (c), 0, 0, 0)
#define SB() __builtin_amdgcn_sched_barrier(0)
#define MEMBAR() asm volatile("" ::: "memory")

constexpr int S = 2048, D = 512, DF = 257;

constexpr size_t WS_EP   = 0;
constexpr size_t WS_FL   = 4096;
constexpr size_t WS_K    = 4096 + 65536;
constexpr size_t WS_V    = WS_K + 65536;
constexpr size_t WS_KP   = WS_V + 65536;
constexpr size_t WS_G    = WS_KP + 4096;
constexpr size_t WS_PM   = WS_G + 8192;
constexpr size_t WS_X1   = WS_PM + 32768;
constexpr size_t WT_STRIDE = 8192 + 256;
constexpr size_t WS_WT   = WS_X1 + (size_t)256 * S * 8;
constexpr size_t Z_STRIDE = 16384 + 256;
constexpr size_t WS_Z    = WS_WT + 256 * WT_STRIDE;
constexpr size_t WS_B    = WS_Z + 256 * Z_STRIDE;
constexpr size_t WS_O    = WS_B + (size_t)S * D * 2;
constexpr size_t WS_END  = WS_O + (size_t)S * D * 2;
constexpr unsigned SENT = 0xFFFFFFFFu;

#define STAMP(k, slot) do {} while (0)
#define TS(i) do {} while (0)
#define TDECL do {} while (0)
#define TDUMP(k) do {} while (0)

DI unsigned pk_bf16(float lo, float hi) { f32x2 f = {lo, hi}; return __builtin_bit_cast(unsigned, __builtin_convertvector(f, bf16x2_t)); }
DI float bf_lo(unsigned u) { return __uint_as_float(u << 16); }
DI float bf_hi(unsigned u) { return __uint_as_float(u & 0xffff0000u); }
DI float bf_f(u16 u) { return __uint_as_float((unsigned)u << 16); }
DI float bfr(float x) { return bf_lo(pk_bf16(x, x)); }

DI void dma16x4(const void* sbase, unsigned v0, unsigned v1, unsigned v2, unsigned v3, unsigned lds_addr) {
  asm volatile("s_mov_b32 m0, %5\n\ts_nop 0\n\tglobal_load_lds_dwordx4 %0, %4\n\tglobal_load_lds_dwordx4 %1, %4 offset:1024\n\t"
               "global_load_lds_dwordx4 %2, %4 offset:2048\n\tglobal_load_lds_dwordx4 %3, %4 offset:3072"
               :: "v"(v0), "v"(v1), "v"(v2), "v"(v3), "s"(sbase), "s"(lds_addr) : "memory");
}
DI void dma16x4p(const void* sbase, unsigned v0, unsigned v1, unsigned v2, unsigned v3, unsigned lds_addr) {
  asm volatile("s_mov_b32 m0, %5\n\ts_nop 0\n\tglobal_load_lds_dwordx4 %0, %4\n\tglobal_load_lds_dwordx4 %1, %4 offset:1040\n\t"
               "global_load_lds_dwordx4 %2, %4 offset:2080\n\tglobal_load_lds_dwordx4 %3, %4 offset:3120"
               :: "v"(v0), "v"(v1), "v"(v2), "v"(v3), "s"(sbase), "s"(lds_addr) : "memory");
}
template <int N> DI void wait_vm() { asm volatile("s_waitcnt vmcnt(%0)" :: "i"(N) : "memory"); }
DI void lds_signal(unsigned addr, int) {
  asm volatile("s_waitcnt lgkmcnt(0)\n\ts_mov_b64 exec, 1\n\tds_add_u32 %0, %1\n\ts_mov_b64 exec, -1" :: "v"(addr), "v"(1u) : "memory");
}
DI void lds_wait(unsigned addr, unsigned n) {
  MEMBAR();
  while ((unsigned)__builtin_amdgcn_readfirstlane(__hip_atomic_load((LAS unsigned*)addr, __ATOMIC_RELAXED, __HIP_MEMORY_SCOPE_WORKGROUP)) < n) __builtin_amdgcn_s_sleep(1);
  MEMBAR();
}
DI unsigned lds_peek(unsigned addr) {
  MEMBAR();
  const unsigned v = __builtin_amdgcn_readfirstlane(__hip_atomic_load((LAS unsigned*)addr, __ATOMIC_RELAXED, __HIP_MEMORY_SCOPE_WORKGROUP));
  MEMBAR();
  return v;
}
DI void lds_wait_slow(unsigned addr, unsigned n) {
  MEMBAR();
  while ((unsigned)__builtin_amdgcn_readfirstlane(__hip_atomic_load((LAS unsigned*)addr, __ATOMIC_RELAXED, __HIP_MEMORY_SCOPE_WORKGROUP)) < n) __builtin_amdgcn_s_sleep(6);
  MEMBAR();
}
DI void wave_sync() { __builtin_amdgcn_fence(__ATOMIC_RELEASE, "wavefront"); __builtin_amdgcn_wave_barrier(); __builtin_amdgcn_fence(__ATOMIC_ACQUIRE, "wavefront"); }
DI void st16_dev(void* p, u32x4 v) { asm volatile("global_store_dwordx4 %0, %1, off sc1\n\ts_nop 1" :: "v"(p), "v"(v) : "memory"); }

DI void st8_dev(void* p, u32x2 v) { asm volatile("global_store_dwordx2 %0, %1, off sc1" :: "v"(p), "v"(v) : "memory"); }
DI void st4_dev(void* p, unsigned v) { asm volatile("global_store_dword %0, %1, off sc1" :: "v"(p), "v"(v) : "memory"); }
#define SC1S " sc1"
DI void dma16x4_dev(const void* sbase, unsigned v0, unsigned lds_addr) {
  asm volatile("s_mov_b32 m0, %2\n\ts_nop 0\n\tglobal_load_lds_dwordx4 %0, %1" SC1S "\n\tglobal_load_lds_dwordx4 %0, %1 offset:1024" SC1S "\n\t"
               "global_load_lds_dwordx4 %0, %1 offset:2048" SC1S "\n\tglobal_load_lds_dwordx4 %0, %1 offset:3072" SC1S
               :: "v"(v0), "s"(sbase), "s"(lds_addr) : "memory");
}
DI unsigned lds_ticket(unsigned addr, int) {
  unsigned t;
  asm volatile("s_waitcnt lgkmcnt(0)\n\ts_mov_b64 exec, 1\n\tds_add_rtn_u32 %0, %1, %2\n\ts_mov_b64 exec, -1\n\ts_waitcnt lgkmcnt(0)" : "=&v"(t) : "v"(addr), "v"(1u) : "memory");
  return __builtin_amdgcn_readfirstlane(t);
}
DI void st16_dev_s(const void* sbase, unsigned voff, u32x4 v) { asm volatile("global_store_dwordx4 %0, %1, %2 sc1\n\ts_nop 1" :: "v"(voff), "v"(v), "s"(sbase) : "memory"); }
template <int CTRL> DI float dppf(float x) { return __int_as_float(__builtin_amdgcn_update_dpp(0, __float_as_int(x), CTRL, 0xf, 0xf, true)); }
DI float x16_sum(float x) { auto r = __builtin_amdgcn_permlane16_swap(__float_as_uint(x), __float_as_uint(x), false, false); return __uint_as_float(r[0]) + __uint_as_float(r[1]); }
DI float x32_sum(float x) { auto r = __builtin_amdgcn_permlane32_swap(__float_as_uint(x), __float_as_uint(x), false, false); return __uint_as_float(r[0]) + __uint_as_float(r[1]); }
DI float x16_max(float x) { auto r = __builtin_amdgcn_permlane16_swap(__float_as_uint(x), __float_as_uint(x), false, false); return fmaxf(__uint_as_float(r[0]), __uint_as_float(r[1])); }
DI float x32_max(float x) { auto r = __builtin_amdgcn_permlane32_swap(__float_as_uint(x), __float_as_uint(x), false, false); return fmaxf(__uint_as_float(r[0]), __uint_as_float(r[1])); }
DI float row16_sum(float x) { x += dppf<0xB1>(x); x += dppf<0x4E>(x); x += dppf<0x141>(x); x += dppf<0x140>(x); return x; }
DI float row16_max(float x) { x = fmaxf(x, dppf<0xB1>(x)); x = fmaxf(x, dppf<0x4E>(x)); x = fmaxf(x, dppf<0x141>(x)); x = fmaxf(x, dppf<0x140>(x)); return x; }
DI float wave_sum(float x) { return x32_sum(x16_sum(row16_sum(x))); }
DI float wave_max(float x) { return x32_max(x16_max(row16_max(x))); }
DI unsigned umax(unsigned a, unsigned b) { return a > b ? a : b; }
DI bool wave_all(bool p) { return __builtin_amdgcn_ballot_w64(!p) == 0ull; }

DI cf cmul(cf a, cf w)  { cf t = a.xx * w; cf u = {-w.y, w.x}; return a.yy * u + t; }
DI cf cmulc(cf a, cf w) { cf t = a * w.xx; cf u = {w.y, -w.y}; return a.yx * u + t; }
template <int INV> DI cf rot90(cf a) { return INV ? cf{-a.y, a.x} : cf{a.y, -a.x}; }
template <int INV> DI cf ctw(cf a, cf w) { return INV ? cmulc(a, w) : cmul(a, w); }
template <int INV> DI void dft4(cf& c0, cf& c1, cf& c2, cf& c3) {
  cf d0 = c0 + c2, d2 = c0 - c2, d1 = c1 + c3, d3 = rot90<INV>(c1 - c3);
  c0 = d0 + d1; c2 = d0 - d1; c1 = d2 + d3; c3 = d2 - d3;
}
template <int INV> DI void dft8(cf (&a)[8]) {
  constexpr float R = 0.70710678118654752440f;
  cf b0 = a[0] + a[4], b4 = a[0] - a[4], b1 = a[1] + a[5], b5 = a[1] - a[5];
  cf b2 = a[2] + a[6], b6 = a[2] - a[6], b3 = a[3] + a[7], b7 = a[3] - a[7];
  cf r5 = rot90<INV>(b5), r7 = rot90<INV>(b7);
  b5 = (b5 + r5) * R;  b6 = rot90<INV>(b6);  b7 = (r7 - b7) * R;
  dft4<INV>(b0, b1, b2, b3);
  dft4<INV>(b4, b5, b6, b7);
  a[0] = b0; a[2] = b1; a[4] = b2; a[6] = b3;
  a[1] = b4; a[3] = b5; a[5] = b6; a[7] = b7;
}
DI cf twid(float rev) { return cf{__builtin_amdgcn_cosf(rev), -__builtin_amdgcn_sinf(rev)}; }
DI cf csq(cf a) { return cf{a.x * a.x - a.y * a.y, 2.f * a.x * a.y}; }
DI void twid7(cf (&w)[8], float rev) {
  w[1] = twid(rev); w[2] = csq(w[1]); w[3] = cmul(w[2], w[1]); w[4] = csq(w[2]); w[5] = cmul(w[4], w[1]); w[6] = csq(w[3]); w[7] = cmul(w[4], w[3]);
}

template <int INV>
DI void fft512_wave(cf (&a)[8], LAS cf* buf  , int lane, const cf (&w1)[8], const cf (&w2)[8]) {
  const int k1 = lane >> 3, n3 = lane & 7;
  dft8<INV>(a);
#pragma unroll
  for (int k = 1; k < 8; ++k) a[k] = ctw<INV>(a[k], w1[k]);
#pragma unroll
  for (int k = 0; k < 8; ++k) buf[k * 72 + lane] = a[k];
  wave_sync();
#pragma unroll
  for (int j = 0; j < 8; ++j) a[j] = buf[k1 * 72 + n3 + 8 * j];
  dft8<INV>(a);
#pragma unroll
  for (int k = 1; k < 8; ++k) a[k] = ctw<INV>(a[k], w2[k]);
  wave_sync();
#pragma unroll
  for (int k = 0; k < 8; ++k) buf[k1 * 72 + 9 * k + n3] = a[k];
  wave_sync();
#pragma unroll
  for (int j = 0; j < 8; ++j) a[j] = buf[k1 * 72 + 9 * n3 + j];
  dft8<INV>(a);
}
DI void pin8(cf (&w)[8]) { asm volatile("" : "+v"(w[1]), "+v"(w[2]), "+v"(w[3]), "+v"(w[4]), "+v"(w[5]), "+v"(w[6]), "+v"(w[7])); }

constexpr unsigned L_X = 0  , L_W = 66560  , L_K = 0, L_V = 65536,
                   L_ONES = 132096, L_ZERO = L_ONES + 2048, L_FFT = L_ZERO + 2048, L_QS = L_FFT + 18688  ,
                   L_SYNC = L_QS + 8192, LDS_BYTES = L_SYNC + 256;
enum { SY_W0 = 0, SY_W1, SY_W2, SY_W3, SY_X, SY_KVPUB, SY_GO1, SY_XWFREE, SY_QS, SY_F1A, SY_F1B, SY_PUB2, SY_GO2, SY_KV0, SY_KV1, SY_FB, SY_PUB3, SY_GO3,
       SY_FFTDONE, SY_PUB4, SY_GO4, SY_PARKED, SY_PARK0, SY_PARK1, SY_PARK2, SY_PARK3, SY_ODONE, SY_TAGOK, SY_XISS, SY_KV2, SY_TAG = 32, SY_OSH = 36   };
#define SY(i) (lb + L_SYNC + (i) * 4)
#define XDELAY lds_wait(SY(SY_KV1), 4)
#define F1PRIO 3
#define GPRIO 3
#define WDELAY lds_wait(SY(SY_XISS), 4)
#define KS 40
constexpr unsigned SPIN_CAP = 1u << 16;

#define EX(i) Sc[i] = __builtin_amdgcn_exp2f(Sc[i]); SB()
#define CV(P, j, i) P[j] = pk_bf16(Sc[i], Sc[i + 1]); SB()
DI void att_step(f32x16& Sc, f32x16& Sn, f32x16& Op, f32x16& Oc, const f32x16& C, const bf16x8& qn, const bf16x8& kn, const bf16x8& v1p, const bf16x8& v0c,
                 u32x4& p1) {
  u32x4 p0;
  Sn = MFMA32(kn, qn, C); SB();
  EX(0); EX(1); EX(2); EX(3); CV(p0, 0, 0); EX(4); CV(p0, 1, 2); EX(5);
  Op = MFMA32(v1p, __builtin_bit_cast(bf16x8, p1), Op); SB();
  EX(6); EX(7); CV(p0, 2, 4); EX(8); CV(p0, 3, 6); EX(9); EX(10);
  Oc = MFMA32(v0c, __builtin_bit_cast(bf16x8, p0), Oc); SB();
  EX(11); CV(p1, 0, 8); EX(12); EX(13); CV(p1, 1, 10); EX(14); EX(15); CV(p1, 2, 12); CV(p1, 3, 14);
}
DI void poll_all(const char* fl, u32x2 tag, int lane) {
  for (unsigned spin = 0; spin < SPIN_CAP; ++spin) {
    u32x4 a, c;
    asm volatile("global_load_dwordx4 %0, %2, %3 sc1\n\tglobal_load_dwordx4 %1, %2, %3 offset:1024 sc1\n\ts_waitcnt vmcnt(0)"
                 : "=&v"(a), "=&v"(c) : "v"(lane * 16), "s"(fl) : "memory");
    const unsigned d = ((a[0] ^ tag[0]) | (a[2] ^ tag[0]) | (c[0] ^ tag[0]) | (c[2] ^ tag[0])) | ((a[1] ^ tag[1]) | (a[3] ^ tag[1]) | (c[1] ^ tag[1]) | (c[3] ^ tag[1]));
    if (wave_all(d == 0u)) break;
    __builtin_amdgcn_s_sleep(2);
  }
}

__attribute__((target("no-packed-fp32-ops"))) __global__ __launch_bounds__(768, 3)
void k_main(const u16* __restrict__ x, const u16* __restrict__ wq, const u16* __restrict__ wk, const u16* __restrict__ wv, const u16* __restrict__ wo,
            const u16* __restrict__ w_r, const u16* __restrict__ w_i, const u16* __restrict__ gw, char* __restrict__ ws, u16* __restrict__ out) {
  extern __shared__ __attribute__((aligned(16))) char lds_[];
  LAS char* lds = (LAS char*)lds_;
  const unsigned lb = (unsigned)(uintptr_t)lds;
  const int tid = threadIdx.x, wid = __builtin_amdgcn_readfirstlane(tid >> 6), lane = tid & 63, b = blockIdx.x;
  const int xg = b & 7, t0 = xg * 32 + (b >> 3), rm = b >> 6, nb = (b >> 3) & 7;
  const int rb0 = xg + 16 * rm;
  const int r = lane & 31, h = lane >> 5;
  TDECL;
  TS(0);
  if (tid < 64) *(LAS unsigned*)(lds + L_SYNC + tid * 4) = 0u;
  else if (tid >= 256) { const unsigned c = tid < 512 ? 0x3f803f80u : 0u; *(LAS u32x2*)(lds + L_ONES + (tid - 256) * 8) = u32x2{c, c}; }
  asm volatile("s_waitcnt lgkmcnt(0)" ::: "memory");
  __builtin_amdgcn_s_barrier();
  MEMBAR();

  if (wid < 8) {
    const int pr = wid & 3, wr = pr >> 1, hp = pr & 1, kh = wid >> 2;
    bf16x8 qA, qB;
    if (kh == 0) {
      {
        unsigned vw[4];
#pragma unroll
        for (int i = 0; i < 4; ++i) vw[i] = (unsigned)((7 * i + (lane >> 3)) * 1024 + (((lane & 7) ^ (((lane >> 4) & 1) << 2)) << 4));
        const char* wb = (const char*)wq + (size_t)wid * 32768 + nb * 128;
        WDELAY;
#pragma unroll
        for (int c = 0; c < 4; ++c) dma16x4(wb + c * 131072, vw[0], vw[1], vw[2], vw[3], lb + L_W + c * 16384 + wid * 4096);
      }
      const int g = lane >> 4, q = (lane & 15) >> 2, p = lane & 3;
      LAS char* wbase = lds + L_W + (8 * h + q) * 128 + ((hp ^ (q >> 1)) * 64) + 32 * (g & 1) + 8 * p;
      LAS char* xbase = lds + L_X + (32 * wr + r) * 1040 + h * 16;
      f32x16 acc = {};
      __builtin_amdgcn_s_setprio(GPRIO);
      wait_vm<8>();  lds_signal(SY(SY_W0), lane);
      wait_vm<0>();  lds_signal(SY(SY_W1), lane);
      lds_wait(SY(SY_X), 4);
      bf16x8 xf[2][4]; s16x4 lo[2][4], hi[2][4];
      auto fetch = [&](int hc, int s_) __attribute__((always_inline)) {
        if ((hc & 3) == 0) lds_wait(SY(SY_W0 + (hc >> 2)), 4);
#pragma unroll
        for (int t = 0; t < 4; ++t) {
          xf[s_][t] = *(LAS bf16x8*)(xbase + (hc * 4 + t) * 32);
          lo[s_][t] = __builtin_amdgcn_ds_read_tr16_b64_v4i16((LAS s16x4*)(wbase + (hc * 4 + t) * 2048));
          hi[s_][t] = __builtin_amdgcn_ds_read_tr16_b64_v4i16((LAS s16x4*)(wbase + (hc * 4 + t) * 2048 + 512));
        }
        SB();
      };
      TS(1);
      fetch(0, 0);
#pragma unroll
      for (int hc = 0; hc < 8; ++hc) {
        if (hc < 7) fetch(hc + 1, (hc + 1) & 1);
#pragma unroll
        for (int t = 0; t < 4; ++t) acc = MFMA32(__builtin_shufflevector(lo[hc & 1][t], hi[hc & 1][t], 0, 1, 2, 3, 4, 5, 6, 7), xf[hc & 1][t], acc);
        SB();
      }
      lds_signal(SY(SY_XWFREE), lane);
      constexpr float C = 0.25f * 1.4426950408889634f;
      u32x4 oa, ob;
#pragma unroll
      for (int i = 0; i < 4; ++i) { oa[i] = pk_bf16(acc[2 * i] * C, acc[2 * i + 1] * C); ob[i] = pk_bf16(acc[8 + 2 * i] * C, acc[9 + 2 * i] * C); }
      qA = __builtin_bit_cast(bf16x8, oa); qB = __builtin_bit_cast(bf16x8, ob);
      lds_wait(SY(SY_KVPUB), 4);
      *(LAS u32x4*)(lds + L_QS + pr * 2048 + lane * 16) = oa;
      *(LAS u32x4*)(lds + L_QS + pr * 2048 + 1024 + lane * 16) = ob;
      lds_signal(SY(SY_QS), lane);
      __builtin_amdgcn_s_setprio(0);
      TS(2);
    } else {
      u32x2 ep;
      __builtin_amdgcn_s_setprio(3);
      {
        const unsigned v0 = 64 + lane * 16;
        const char* xb = (const char*)x + (size_t)((rb0 + 8 * wr) * 32 + hp * 16) * 1024 - 64;
#pragma unroll
        for (int gi = 0; gi < 4; ++gi) dma16x4p(xb + gi * 4096, v0, v0 - 16, v0 - 32, v0 - 48, lb + L_X + (pr * 16 + gi * 4) * 1040);
        u32x2 kv0, kv1;
        const u16* kvs = (nb < 4 ? wk : wv) + 4 * (nb & 3) + pr * 2048;
        asm volatile("global_load_dwordx2 %0, %3, %4\n\tglobal_load_dwordx2 %1, %3, %4 offset:2048\n\tglobal_load_dwordx2 %2, %5, %6 sc1"
                     : "=&v"(kv0), "=&v"(kv1), "=&v"(ep) : "v"(lane * 32), "s"(kvs), "v"(0), "s"(ws + WS_EP) : "memory");
        lds_signal(SY(SY_XISS), lane);
        asm volatile("s_waitcnt vmcnt(0)" : "+v"(kv0), "+v"(kv1), "+v"(ep) :: "memory");
        *(LAS u32x2*)(lds + L_QS + pr * 1024 + lane * 8) = kv0;
        *(LAS u32x2*)(lds + L_QS + pr * 1024 + 512 + lane * 8) = kv1;
        lds_signal(SY(SY_X), lane);
      }
      const u32x4 gq = *(const u32x4*)(gw + lane * 8);
      lds_wait(SY(SY_X), 4);
      TS(1);
      {
        const int g = lane >> 4, q = (lane & 15) >> 2, row2 = 16 * pr + (lane & 15);
        LAS char* ab = lds + L_QS + (8 * g + q) * 8;
        LAS char* xb2 = lds + L_X + row2 * 1040 + g * 16;
        f32x4 acc2 = {};
        const int tile = rb0 + 8 * wr;
        bf16x8 yf[16], af[16];
#pragma unroll
        for (int t = 0; t < 16; ++t) {
          yf[t] = *(LAS bf16x8*)(xb2 + t * 64);
          const s16x4 l2 = __builtin_amdgcn_ds_read_tr16_b64_v4i16((LAS s16x4*)(ab + t * 256)), h2 = __builtin_amdgcn_ds_read_tr16_b64_v4i16((LAS s16x4*)(ab + t * 256 + 32));
          af[t] = __builtin_shufflevector(l2, h2, 0, 1, 2, 3, 4, 5, 6, 7);
        }
        SB();
        if (nb < 4) {
#pragma unroll
          for (int t = 0; t < 16; ++t) acc2 = MFMA16(af[t], yf[t], acc2);
          const unsigned p0 = pk_bf16(acc2[0], acc2[1]), p1 = pk_bf16(acc2[2], acc2[3]);
          if (lane < 16) st8_dev(ws + WS_K + tile * 1024 + (nb & 1) * 512 + (row2 & 31) * 16 + (nb >> 1) * 8, u32x2{p0, p1});
          float n2 = bf_lo(p0) * bf_lo(p0) + bf_hi(p0) * bf_hi(p0) + bf_lo(p1) * bf_lo(p1) + bf_hi(p1) * bf_hi(p1);
          n2 = row16_max(n2);
          if (lane == 0) st4_dev(ws + WS_KP + ((tile * 2 + hp) * 4 + nb) * 4, __builtin_bit_cast(unsigned, n2));
        } else {
#pragma unroll
          for (int t = 0; t < 16; ++t) acc2 = MFMA16(yf[t], af[t], acc2);
          if ((lane & 15) < 4)
            st8_dev(ws + WS_V + tile * 1024 + hp * 512 + (g & 1) * 256 + ((nb - 4) * 4 + (lane & 15)) * 16 + (g >> 1) * 8, u32x2{pk_bf16(acc2[0], acc2[1]), pk_bf16(acc2[2], acc2[3])});
        }
      }
      const int r0 = 8 * nb + 2 * pr;
      const u32x4 xa = *(LAS u32x4*)(lds + L_X + r0 * 1040 + lane * 16);
      const u32x4 xb = *(LAS u32x4*)(lds + L_X + (r0 + 1) * 1040 + lane * 16);
      wait_vm<0>();
      lds_signal(SY(SY_XWFREE), lane);
      const unsigned tk = lds_ticket(SY(SY_KVPUB), lane);
      TS(2);
      u32x2 tag;
      { const unsigned long long e = (((unsigned long long)ep[1] << 32) | ep[0]) + 1ull; tag = u32x2{(unsigned)e, (unsigned)(e >> 32)}; }
      if (tk == 3) {
        if (lane < 8) st8_dev(ws + WS_FL + lane * 2048 + b * 8, tag);
        if (lane == 0) *(LAS u32x2*)(lds + L_SYNC + SY_TAG * 4) = tag;
        lds_signal(SY(SY_TAGOK), lane);
      }
      {
        float gs1 = 0.f, gs2 = 0.f;
#pragma unroll
        for (int i = 0; i < 4; ++i) {
          const unsigned pa = pk_bf16(bf_lo(xa[i]) * bf_lo(gq[i]), bf_hi(xa[i]) * bf_hi(gq[i])), pb = pk_bf16(bf_lo(xb[i]) * bf_lo(gq[i]), bf_hi(xb[i]) * bf_hi(gq[i]));
          gs1 += bf_lo(pa) + bf_hi(pa); gs2 += bf_lo(pb) + bf_hi(pb);
        }
        gs1 = wave_sum(gs1); gs2 = wave_sum(gs2);
        if (lane < 2) {
          const float z = bfr(lane ? gs2 : gs1);
          st4_dev(ws + WS_G + ((rb0 + 8 * (r0 >> 5)) * 32 + (r0 & 31) + lane) * 4,
                  __builtin_bit_cast(unsigned, bfr(__builtin_amdgcn_rcpf(1.0f + __builtin_amdgcn_exp2f(z * -1.4426950408889634f)))));
        }
      }
      wait_vm<0>();
      lds_signal(SY(SY_PUB2), lane);
      if (tk == 3) {
        poll_all(ws + WS_FL + xg * 2048, tag, lane);
        lds_signal(SY(SY_GO1), lane);
        if (b == 0 && lane == 0) st8_dev(ws + WS_EP, tag);
      }
      __builtin_amdgcn_s_setprio(0);
      lds_wait(SY(SY_QS), 4);
      qA = *(LAS bf16x8*)(lds + L_QS + pr * 2048 + lane * 16);
      qB = *(LAS bf16x8*)(lds + L_QS + pr * 2048 + 1024 + lane * 16);
    }
    float qn2;
    {
      const u32x4 qu = __builtin_bit_cast(u32x4, qA), qv = __builtin_bit_cast(u32x4, qB);
      float na = 0.f, nq = 0.f;
#pragma unroll
      for (int i = 0; i < 4; ++i) { float a = bf_lo(qu[i]), c = bf_hi(qu[i]); na += a * a + c * c; a = bf_lo(qv[i]); c = bf_hi(qv[i]); nq += a * a + c * c; }
      qn2 = fmaxf(x32_sum(na), x32_sum(nq));
    }
    lds_wait(SY(SY_XWFREE), 8);
    lds_wait(SY(SY_GO1), 1);
    TS(3);
    f32x4 kp0, kp1;
    asm volatile("global_load_dwordx4 %0, %2, %3 sc1\n\tglobal_load_dwordx4 %1, %2, %3 offset:1024 sc1" : "=&v"(kp0), "=&v"(kp1) : "v"(lane * 16), "s"(ws + WS_KP) : "memory");
    {
      const int off = (wr * 64 + hp * KS + kh * 2) * 1024;
      asm volatile("s_mov_b32 m0, %2\n\ts_nop 0\n\tglobal_load_lds_dwordx4 %0, %1 sc1\n\tglobal_load_lds_dwordx4 %0, %1 offset:1024 sc1"
                   :: "v"(lane * 16), "s"(ws + WS_K + off), "s"(lb + off) : "memory");
    }
    asm volatile("s_waitcnt vmcnt(2)" : "+v"(kp0), "+v"(kp1) :: "memory");
    const float km = wave_max(fmaxf(kp0[0] + kp0[1] + kp0[2] + kp0[3], kp1[0] + kp1[1] + kp1[2] + kp1[3]));
    const float negm = -__builtin_amdgcn_sqrtf(qn2 * km) * 1.0001f;
    LAS char* kp = lds + L_K + lane * 16;
    const int vstep = (r < 16) ? 2048 : 0;
    const bool onesrow = (r & 27) == 16;
    f32x16 OA = {}, OB = {};
    {
      f32x16 C;
#pragma unroll
      for (int i = 0; i < 16; ++i) C[i] = negm;
      int va = (r < 16) ? (L_V + kh * (KS * 1024) + h * 256 + r * 16) : ((onesrow ? L_ONES : L_ZERO) + h * 256 + (r - 16) * 16);
      int ka = kh * (KS * 1024);
      wait_vm<0>();
      lds_signal(SY(SY_KV0), lane);
      lds_wait(SY(SY_KV0), 8);
      if (kh == 1) {
#pragma unroll
        for (int i = 0; i < 7; ++i) {
          const int tl = i < 2 ? hp * KS + 4 + 4 * i : (hp == 0 ? 4 + 4 * i : (32 + 4 * (i - 2) < KS ? 32 + 4 * (i - 2) : 44 + 4 * (i - 2)));
          const int off = (wr * 64 + tl) * 1024;
          dma16x4_dev(ws + WS_K + off, lane * 16, lb + off);
        }
        wait_vm<20>(); lds_signal(SY(SY_KV1), lane);
        wait_vm<0>();  lds_signal(SY(SY_KV2), lane);
      }
      TS(4);
#define LDK(o) (*(LAS bf16x8*)(kp + ka + (o)))
#define LDV(o) (*(LAS bf16x8*)(lds + va + (o)))
      bf16x8 k0 = LDK(0), k1, v00 = LDV(0), v10 = LDV(512), v01, v11 = v10;
      f32x16 Sa = MFMA32(k0, qA, C), Sb;
      u32x4 p1 = {0u, 0u, 0u, 0u};
#pragma unroll 1
      for (int ph = 0; ph < 3; ++ph) {
        const int nit = ph == 0 ? 1 : (ph == 1 ? 4 : (kh ? (64 - KS) / 2 - 5 : KS / 2 - 5));
#pragma unroll 1
        for (int it = 0; it < nit; ++it) {
          k1 = LDK(1024); v01 = LDV(1024); SB();
          att_step(Sa, Sb, OB, OA, C, qB, k0, v11, v00, p1);
          v11 = LDV(1024 + 512); SB();
          att_step(Sb, Sa, OA, OB, C, qA, k1, v10, v00, p1);
          va += vstep;
          k0 = LDK(2048); v00 = LDV(0); SB();
          att_step(Sa, Sb, OB, OA, C, qB, k1, v10, v01, p1);
          v10 = LDV(512); SB();
          att_step(Sb, Sa, OA, OB, C, qA, k0, v11, v01, p1);
          ka += 2048;
        }
        if (ph < 2) lds_wait(SY(SY_KV1) + ph * (SY_KV2 - SY_KV1) * 4, 4);
      }
      OB = MFMA32(v11, __builtin_bit_cast(bf16x8, p1), OB);
    }
    TS(5);
    LAS float* red = (LAS float*)(lds + L_K + pr * 4608) + lane;
    const int rbw = rb0 + 8 * wr;
    if (kh == 0) {
      lds_signal(SY(SY_PARKED), lane);
      lds_wait(SY(SY_PARKED), 4);
      {
        unsigned vw[4];
#pragma unroll
        for (int i = 0; i < 4; ++i) vw[i] = (unsigned)((7 * i + (lane >> 3)) * 1024 + (((lane & 7) ^ (((lane >> 4) & 1) << 2)) << 4));
        const char* wb = (const char*)wo + (size_t)pr * 131072 + nb * 128;
        const unsigned wd = lb + (wr ? L_V : L_K) + hp * 16384;
#pragma unroll
        for (int c = 0; c < 4; ++c) { dma16x4(wb + c * 32768, vw[0], vw[1], vw[2], vw[3], wd + c * 4096); if (c & 1) wait_vm<0>(); }
        lds_signal(SY(SY_PARKED), lane);
        lds_wait(SY(SY_PARKED), 8);
      }
      bf16x8 wf[32];
      {
        const int g = lane >> 4, q = (lane & 15) >> 2, p = lane & 3;
        const unsigned wl = (unsigned)((4 * h + q) * 128 + ((hp ^ (q >> 1)) * 64) + 32 * (g & 1) + 8 * p);
#pragma unroll
        for (int k4 = 0; k4 < 8; ++k4) {
          const int hd = (nb + k4) & 7;
          LAS char* src = lds + wl + (hd >> 2) * L_V + (hd & 3) * 8192;
#pragma unroll
          for (int e = 0; e < 4; ++e) {
            const s16x4 lo = __builtin_amdgcn_ds_read_tr16_b64_v4i16((LAS s16x4*)(src + e * 2048)), hi = __builtin_amdgcn_ds_read_tr16_b64_v4i16((LAS s16x4*)(src + e * 2048 + 1024));
            wf[4 * k4 + e] = __builtin_shufflevector(lo, hi, 0, 1, 2, 3, 4, 5, 6, 7);
          }
        }
      }
      lds_signal(SY(SY_PARKED), lane);
      lds_wait(SY(SY_PARKED), 12);
#pragma unroll
      for (int i = 0; i < 9; ++i) { red[i * 64] = OA[i]; red[(9 + i) * 64] = OB[i]; }
      lds_signal(SY(SY_PARK0 + pr), lane);
      TS(8);
      const int nt = nb * 2 + hp;
      const u32x2 tag = *(LAS u32x2*)(lds + L_SYNC + SY_TAG * 4);
      LAS char* bs = lds + L_FFT + pr * 4608 + lane * 16;
      bool haveb = false;
      auto try_b = [&]() __attribute__((always_inline)) {
        u32x2 f;
        asm volatile("global_load_dwordx2 %0, %1, %2 sc1\n\ts_waitcnt vmcnt(0)" : "=&v"(f) : "v"((lane & 7) * 8), "s"(ws + WS_FL + 49152 + (xg * 4 + rm) * 256) : "memory");
        if (wave_all(f[0] == tag[0] && f[1] == tag[1])) {
          u32x4 b0, b1; float g;
          asm volatile("global_load_dwordx4 %0, %3, %4 sc1\n\tglobal_load_dwordx4 %1, %3, %4 offset:16 sc1\n\tglobal_load_dword %2, %5, %6 sc1\n\ts_waitcnt vmcnt(0)"
                       : "=&v"(b0), "=&v"(b1), "=&v"(g) : "v"(lane * 32), "s"(ws + WS_B + ((size_t)rbw * 16 + nt) * 2048), "v"(r * 4), "s"(ws + WS_G + rbw * 128) : "memory");
          const float omg = bfr(1.0f - g);
#pragma unroll
          for (int i = 0; i < 4; ++i) { b0[i] = pk_bf16(omg * bf_lo(b0[i]), omg * bf_hi(b0[i])); b1[i] = pk_bf16(omg * bf_lo(b1[i]), omg * bf_hi(b1[i])); }
          *(LAS u32x4*)bs = b0; *(LAS u32x4*)(bs + 1024) = b1; *(LAS float*)(bs + 2048) = g;
          haveb = true;
        }
      };
      lds_wait_slow(SY(SY_PUB4), 4);
      while (!haveb && lds_peek(SY(SY_ODONE)) < 4u) { try_b(); if (!haveb) __builtin_amdgcn_s_sleep(4); }
      TS(9);
      lds_wait_slow(SY(SY_ODONE), 2);
      lds_wait(SY(SY_ODONE), 4);
      f32x16 acc = {};
#pragma unroll
      for (int k = 0; k < 4; ++k) acc = MFMA32(wf[k], *(LAS bf16x8*)(lds + L_QS + (wr * 4 + k) * 1024 + lane * 16), acc);
      {
        LAS char* of = lds + (wr ? L_V : L_K) + lane * 16;
        lds_wait(SY(SY_OSH + wr * 2), 1);
#pragma unroll
        for (int k = 4; k < 18; ++k) { acc = MFMA32(wf[k], *(LAS bf16x8*)(of + (k - 4) * 1024), acc); if (k == 8 || k == 13) SB(); }
        lds_wait(SY(SY_OSH + wr * 2 + 1), 1);
        TS(10);
#pragma unroll
        for (int k = 18; k < 32; ++k) { acc = MFMA32(wf[k], *(LAS bf16x8*)(of + (k - 4) * 1024), acc); if (k == 24) SB(); }
      }
      for (unsigned spin = 0; !haveb && spin < SPIN_CAP; ++spin) try_b();
      const u32x4 b0 = *(LAS u32x4*)bs, b1 = *(LAS u32x4*)(bs + 1024); const float g = *(LAS float*)(bs + 2048);
      u16* op = out + (size_t)(rbw * 32 + r) * D + nt * 32 + 4 * h;
#pragma unroll
      for (int q = 0; q < 4; ++q) {
        const unsigned t01 = q < 2 ? b0[2 * q] : b1[2 * q - 4], t23 = q < 2 ? b0[2 * q + 1] : b1[2 * q - 3];
        const unsigned a01 = pk_bf16(acc[4 * q], acc[4 * q + 1]), a23 = pk_bf16(acc[4 * q + 2], acc[4 * q + 3]);
        const unsigned g01 = pk_bf16(g * bf_lo(a01), g * bf_hi(a01)), g23 = pk_bf16(g * bf_lo(a23), g * bf_hi(a23));
        *(u32x2*)(op + 8 * q) = u32x2{pk_bf16(bf_lo(g01) + bf_lo(t01), bf_hi(g01) + bf_hi(t01)), pk_bf16(bf_lo(g23) + bf_lo(t23), bf_hi(g23) + bf_hi(t23))};
      }
      TS(6);
      TDUMP(0);
      return;
    }
    lds_wait(SY(SY_PUB3), 4);
    lds_wait(SY(SY_PARK0 + pr), 1);
#pragma unroll
    for (int i = 0; i < 9; ++i) { OA[i] += red[i * 64]; OB[i] += red[(9 + i) * 64]; }
    if (!wave_all(OA[8] >= 1.2621774483536189e-29f && OB[8] >= 1.2621774483536189e-29f)) {
      __builtin_amdgcn_fence(__ATOMIC_ACQUIRE, "agent");
      const bf16x8* kg = (const bf16x8*)(ws + WS_K) + lane;
      const bf16x8* vg = (const bf16x8*)(ws + WS_V + h * 256 + (r & 15) * 16);
      const short cv = onesrow ? (short)0x3f80 : (short)0;
      const bf16x8 vc = {cv, cv, cv, cv, cv, cv, cv, cv};
#pragma unroll 1
      for (int hh = 0; hh < 2; ++hh) {
        const bf16x8 qf = hh ? qB : qA;
        float m = -__builtin_inff();
        f32x16 C = {};
#pragma unroll 1
        for (int t = 0; t < 64; ++t) {
          f32x16 Sx = MFMA32(kg[t * 64], qf, C);
#pragma unroll
          for (int i = 0; i < 16; ++i) m = fmaxf(m, Sx[i]);
        }
        m = x32_max(m);
#pragma unroll
        for (int i = 0; i < 16; ++i) C[i] = -m;
        f32x16 O = {};
#pragma unroll 1
        for (int t = 0; t < 64; ++t) {
          f32x16 Sx = MFMA32(kg[t * 64], qf, C);
          const bf16x8 va0 = vg[t * 64], va1 = vg[t * 64 + 32];
          u32x4 p0, p1;
#pragma unroll
          for (int i = 0; i < 4; ++i) {
            p0[i] = pk_bf16(__builtin_amdgcn_exp2f(Sx[2 * i]), __builtin_amdgcn_exp2f(Sx[2 * i + 1]));
            p1[i] = pk_bf16(__builtin_amdgcn_exp2f(Sx[8 + 2 * i]), __builtin_amdgcn_exp2f(Sx[9 + 2 * i]));
          }
          O = MFMA32(r < 16 ? va0 : vc, __builtin_bit_cast(bf16x8, p0), O);
          O = MFMA32(r < 16 ? va1 : vc, __builtin_bit_cast(bf16x8, p1), O);
        }
        if (hh) OB = O; else OA = O;
      }
    }
    {
      const float ia = __builtin_amdgcn_rcpf(OA[8]), ib = __builtin_amdgcn_rcpf(OB[8]);
      u32x4 fa, fb;
#pragma unroll
      for (int i = 0; i < 4; ++i) {
        fa[i] = pk_bf16(OA[2 * i] * ia, OA[2 * i + 1] * ia); fb[i] = pk_bf16(OB[2 * i] * ib, OB[2 * i + 1] * ib);
        if (fa[i] == SENT) fa[i] = 0x7fc07fc0u;
        if (fb[i] == SENT) fb[i] = 0x7fc07fc0u;
      }
      char* og = ws + WS_O + ((size_t)(rbw * 32 + nb * 4 + hp * 2) * 64 + lane) * 16;
      st16_dev(og, fa); st16_dev(og + 1024, fb);
      *(LAS u32x4*)(lds + L_QS + pr * 2048 + lane * 16) = fa;
      *(LAS u32x4*)(lds + L_QS + pr * 2048 + 1024 + lane * 16) = fb;
      lds_signal(SY(SY_ODONE), lane);
    }
    TS(6);
    TDUMP(1);
    return;
  }

  __builtin_amdgcn_s_setprio(F1PRIO);
  const int w = wid - 8, t = tid - 512;
  LAS cf* buf = (LAS cf*)(lds + L_FFT);
  LAS cf* wbuf = buf + w * 576;
  const int k1n = lane & 7, in_ = (lane >> 3) + 8 * w;
  const int k1 = t >> 5, k2 = (t >> 2) & 7, ip = t & 3;
  cf twR1[8], twR2[8], twA[8], twB[8], twC[8];
  {
    const int sA = t0 + 512 * w;
    const u32x4 xa = *(const u32x4*)(x + (size_t)sA * D + lane * 8), xb = *(const u32x4*)(x + (size_t)(sA + 256) * D + lane * 8);
    twid7(twR1, (float)lane * (1.0f / 512.0f));
    twid7(twR2, (float)(lane & 7) * (1.0f / 64.0f));
    *(LAS u32x4*)((LAS char*)wbuf + lane * 16) = xa;
    *(LAS u32x4*)((LAS char*)wbuf + 1024 + lane * 16) = xb;
    wave_sync();
    cf a[8];
#pragma unroll
    for (int j = 0; j < 8; ++j) a[j] = cf{bf_f(((LAS u16*)wbuf)[lane + 64 * j]), bf_f(((LAS u16*)wbuf)[512 + lane + 64 * j])};
    wave_sync();
    fft512_wave<0>(a, wbuf, lane, twR1, twR2);
    const int ub = (t0 >> 5) + 8 * ((t0 >> 2) & 7), vb = (t0 & 3) * 64 + w * 16;
    typedef unsigned u32x4_u __attribute__((ext_vector_type(4), aligned(2)));
    const size_t wofs = (size_t)(ub + 64 * (lane >> 1)) * DF + vb + (lane & 1) * 8;
    const u32x4 fr = *(const u32x4_u*)(w_r + wofs), fi = *(const u32x4_u*)(w_i + wofs);
    u16 wov[8] = {0, 0, 0, 0, 0, 0, 0, 0};
    if (t >= 128 && t < 136) {
      const int j = t - 128, u = ub + 128 * (t0 & 3) + 64 * (j >> 2) + 512 * (j & 3), un = (S - u) & (S - 1);
      wov[0] = w_r[u * DF]; wov[1] = w_i[u * DF]; wov[2] = w_r[un * DF]; wov[3] = w_i[un * DF];
      wov[4] = w_r[u * DF + 256]; wov[5] = w_i[u * DF + 256]; wov[6] = w_r[un * DF + 256]; wov[7] = w_i[un * DF + 256];
    }
    const int kl = (lane >> 3) + 8 * (lane & 7);
    wave_sync();
#pragma unroll
    for (int k = 0; k < 8; ++k) wbuf[k * 64 + kl] = a[k];
    lds_signal(SY(SY_F1A), lane);
    cf tw[8]; twid7(tw, (float)t0 * (1.0f / 2048.0f));
    constexpr float SC = 1.0f / 2097152.0f;
    lds_wait(SY(SY_F1A), 4);
    {
      const int mi = t ? 512 - t : 256;
#pragma unroll
      for (int q = 0; q < 4; ++q) {
        const cf A = buf[q * 576 + t] * SC, B = buf[q * 576 + mi] * SC;
        a[2 * q]     = t ? cf{A.x + B.x, A.y - B.y} : cf{2.f * A.x, 2.f * B.x};
        a[2 * q + 1] = t ? cf{A.y + B.y, B.x - A.x} : cf{2.f * A.y, 2.f * B.y};
      }
    }
    lds_signal(SY(SY_F1B), lane);
    dft8<0>(a);
#pragma unroll
    for (int k = 1; k < 8; ++k) a[k] = cmul(a[k], tw[k]);
    lds_wait(SY(SY_F1B), 4);
#pragma unroll
    for (int q = 0; q < 4; ++q) ((LAS f32x4*)wbuf)[lane * 4 + (q ^ ((lane >> 1) & 3))] = f32x4{a[2 * q].x, a[2 * q].y, a[2 * q + 1].x, a[2 * q + 1].y};
    wave_sync();
    u32x4 xs[4], wt2[2];
#pragma unroll
    for (int m = 0; m < 4; ++m) { const int p = m * 64 + lane; xs[m] = ((LAS u32x4*)wbuf)[p ^ ((p >> 3) & 3)]; }
    TS(1);
    wave_sync();
    {
      LAS char* st = (LAS char*)wbuf;
      LAS unsigned* d0 = (LAS unsigned*)(st + (lane >> 1) * 36 + (lane & 1) * 16);
#pragma unroll
      for (int i = 0; i < 4; ++i) { d0[i] = fr[i]; d0[288 + i] = fi[i]; }
      wave_sync();
#pragma unroll
      for (int n = 0; n < 2; ++n) {
        const LAS u16* sr = (const LAS u16*)(st + (lane & 7) * 36) + (lane >> 3) + 8 * n;
#pragma unroll
        for (int i = 0; i < 4; ++i) wt2[n][i] = (unsigned)sr[i * 144] | ((unsigned)sr[576 + i * 144] << 16);
      }
    }
    u32x4 wofr;
    {
      const cf h0 = {bf_f(wov[0]) + bf_f(wov[2]), bf_f(wov[1]) - bf_f(wov[3])}, h1 = {bf_f(wov[4]) + bf_f(wov[6]), bf_f(wov[5]) - bf_f(wov[7])};
      const cf P = (h0 + h1) * 0.25f, M = (h0 - h1) * 0.25f;
      wofr = __builtin_bit_cast(u32x4, f32x4{P.x, P.y, M.x, M.y});
    }
    twid7(twB, (float)in_ * (1.0f / 256.0f));
    twid7(twC, (float)ip * (1.0f / 32.0f));
    twid7(twA, (float)t * (1.0f / 2048.0f));
    pin8(twA); pin8(twB); pin8(twC); pin8(twR1); pin8(twR2);
    TS(2);
    XDELAY;
    {
#pragma unroll
      for (int m = 0; m < 4; ++m) st16_dev_s(ws + WS_X1 + (size_t)t0 * 16384 + m * 1024, w * 4096 + lane * 16, xs[m]);
#pragma unroll
      for (int n = 0; n < 2; ++n) st16_dev(ws + WS_WT + (size_t)(vb + (lane >> 3) + 8 * n) * WT_STRIDE + (t0 >> 2) * 128 + (lane & 7) * 16, wt2[n]);
      if (t >= 128 && t < 136) st16_dev(ws + WS_PM + ((t - 128) * 256 + t0) * 16, wofr);
    }
    wait_vm<0>();
    lds_signal(SY(SY_PUB2), lane);
  }
  TS(8);
  lds_wait(SY(SY_TAGOK), 1);
  const u32x2 tag = *(LAS u32x2*)(lds + L_SYNC + SY_TAG * 4);
  if (w == 0) {
    lds_wait(SY(SY_PUB2), 8);
    if (lane < 8) st8_dev(ws + WS_FL + 16384 + lane * 2048 + b * 8, tag);
    poll_all(ws + WS_FL + 16384 + xg * 2048, tag, lane);
    lds_signal(SY(SY_GO2), lane);
  }
  unsigned fbn = 0;
#define FFT_BAR() do { fbn += 4; lds_signal(SY(SY_FB), lane); lds_wait(SY(SY_FB), fbn); } while (0)
  {
    const int v = t0;
    LAS cf* bN = buf + k1n * 292 + in_;
    LAS cf* bO = buf + k1 * 292 + k2 * 36 + ip;
    LAS cf* bP = buf + k1 * 292 + k2 * 36 + ip * 8;
    LAS cf* bA = buf + (t >> 5) * 36 + (t & 31);
    lds_wait(SY(SY_GO2), 1);
    TS(3);
    cf a[8]; u32x4 wv0, wv1;
    {
      const unsigned xo = (unsigned)((in_ * 256 + v) * 64 + k1n * 8);
      const char* xb = ws + WS_X1;
#define LDX(j) asm volatile("global_load_dwordx2 %0, %1, %2 sc1" : "=&v"(a[j]) : "v"(xo), "s"(xb + (size_t)(j) * 524288) : "memory")
      LDX(0); LDX(4); LDX(1); LDX(5); LDX(2); LDX(6); LDX(3); LDX(7);
      const char* wt = ws + WS_WT + (size_t)v * WT_STRIDE;
      asm volatile("global_load_dwordx4 %0, %2, %3 sc1\n\tglobal_load_dwordx4 %1, %2, %3 offset:16 sc1" : "=&v"(wv0), "=&v"(wv1) : "v"(t * 32), "s"(wt) : "memory");
    }
    asm volatile("s_waitcnt vmcnt(0)" : "+v"(a[0]), "+v"(a[1]), "+v"(a[2]), "+v"(a[3]), "+v"(a[4]), "+v"(a[5]), "+v"(a[6]), "+v"(a[7]), "+v"(wv0), "+v"(wv1) :: "memory");
    f32x4 pm[8];
    if (v == 0) {
#define LDP(j) asm volatile("global_load_dwordx4 %0, %1, %2 sc1" : "=&v"(pm[j]) : "v"(t * 16), "s"(ws + WS_PM + (j) * 4096) : "memory")
      LDP(0); LDP(1); LDP(2); LDP(3); LDP(4); LDP(5); LDP(6); LDP(7);
    }
    dft8<0>(a);
#pragma unroll
    for (int k = 1; k < 8; ++k) a[k] = cmul(a[k], twB[k]);
#pragma unroll
    for (int k = 0; k < 8; ++k) bN[36 * k] = a[k];
    FFT_BAR();
#pragma unroll
    for (int j = 0; j < 8; ++j) a[j] = bO[4 * j];
    dft8<0>(a);
#pragma unroll
    for (int k = 1; k < 8; ++k) a[k] = cmul(a[k], twC[k]);
#pragma unroll
    for (int k = 0; k < 8; ++k) bO[4 * k] = a[k];
    wave_sync();
#pragma unroll
    for (int j = 0; j < 8; ++j) a[j] = bP[j];
    dft4<0>(a[0], a[1], a[2], a[3]);
    dft4<0>(a[4], a[5], a[6], a[7]);
    if (v != 0) {
#pragma unroll
      for (int j = 0; j < 4; ++j) { a[j] = cmul(a[j], cf{bf_lo(wv0[j]), bf_hi(wv0[j])}); a[4 + j] = cmul(a[4 + j], cf{bf_lo(wv1[j]), bf_hi(wv1[j])}); }
    } else {
#pragma unroll
      for (int j = 0; j < 8; ++j) bP[j] = a[j];
      asm volatile("s_waitcnt vmcnt(0)" : "+v"(pm[0]), "+v"(pm[1]), "+v"(pm[2]), "+v"(pm[3]), "+v"(pm[4]), "+v"(pm[5]), "+v"(pm[6]), "+v"(pm[7]) :: "memory");
      FFT_BAR();
#pragma unroll
      for (int j = 0; j < 8; ++j) {
        const int un = (S - (k1 + 8 * k2 + 64 * (2 * ip + (j >> 2)) + 512 * (j & 3))) & (S - 1);
        cf cn = buf[(un & 7) * 292 + ((un >> 3) & 7) * 36 + ((un >> 6) & 7) * 4 + (un >> 9)]; cn.y = -cn.y;
        a[j] = cmul(a[j], cf{pm[j].x, pm[j].y}) + cmul(cn, cf{pm[j].z, pm[j].w});
      }
      FFT_BAR();
    }
    dft4<1>(a[0], a[1], a[2], a[3]);
    dft4<1>(a[4], a[5], a[6], a[7]);
#pragma unroll
    for (int j = 0; j < 8; ++j) bP[j] = a[j];
    wave_sync();
#pragma unroll
    for (int k = 0; k < 8; ++k) a[k] = bO[4 * k];
#pragma unroll
    for (int k = 1; k < 8; ++k) a[k] = cmulc(a[k], twC[k]);
    dft8<1>(a);
#pragma unroll
    for (int j = 0; j < 8; ++j) bO[4 * j] = a[j];
    FFT_BAR();
#pragma unroll
    for (int k = 0; k < 8; ++k) a[k] = bN[36 * k];
#pragma unroll
    for (int k = 1; k < 8; ++k) a[k] = cmulc(a[k], twB[k]);
    dft8<1>(a);
#pragma unroll
    for (int j = 0; j < 8; ++j) bN[36 * j] = a[j];
    FFT_BAR();
#pragma unroll
    for (int k = 0; k < 8; ++k) a[k] = bA[292 * k];
#pragma unroll
    for (int k = 1; k < 8; ++k) a[k] = cmulc(a[k], twA[k]);
    dft8<1>(a);
#pragma unroll
    for (int j = 0; j < 8; ++j) bA[292 * j] = a[j];
    wave_sync();
    {
      const int te = t & ~1, jo = (t & 1) * 4;
      const unsigned zo = (unsigned)(((te >> 3) + jo * 32) * Z_STRIDE + (te & 7) * 8);
      const LAS cf* src = bA - (t & 1) + 292 * jo;
#pragma unroll
      for (int j = 0; j < 4; ++j) st16_dev_s(ws + WS_Z + (size_t)j * 32 * Z_STRIDE + v * 64, zo, *(const LAS u32x4*)(src + 292 * j));
    }
    {
      const char* of = ws + WS_O + ((size_t)(rb0 + 8 * (w >> 1)) * 32 + nb * 4) * 1024 + (w & 1) * 1024;
      st16_dev_s(of, lane * 16, u32x4{SENT, SENT, SENT, SENT}); st16_dev_s(of + 2048, lane * 16, u32x4{SENT, SENT, SENT, SENT});
    }
    wait_vm<0>();
    TS(4);
    if (lds_ticket(SY(SY_PUB3), lane) == 3) {
      if (lane < 8) st8_dev(ws + WS_FL + 32768 + lane * 2048 + b * 8, tag);
      poll_all(ws + WS_FL + 32768 + xg * 2048, tag, lane);
      lds_signal(SY(SY_GO3), lane);
    }
    lds_wait_slow(SY(SY_GO3), 1);
  }
  TS(5);
  {
    const int rbf = rb0 + 8 * (nb >> 2), sblk = rbf * 4 + (nb & 3);
    cf a[8];
    {
      const char* z = ws + WS_Z + (size_t)sblk * Z_STRIDE + w * 16;
      const unsigned o1 = lane * 64, o2 = ((64 - lane) & 63) * 64 + (lane ? 0 : 4096);
      f32x4 q[8];
      asm volatile("global_load_dwordx4 %0, %8, %10 sc1\n\tglobal_load_dwordx4 %1, %8, %11 sc1\n\tglobal_load_dwordx4 %2, %8, %12 sc1\n\tglobal_load_dwordx4 %3, %8, %13 sc1\n\t"
                   "global_load_dwordx4 %4, %9, %13 sc1\n\tglobal_load_dwordx4 %5, %9, %12 sc1\n\tglobal_load_dwordx4 %6, %9, %11 sc1\n\tglobal_load_dwordx4 %7, %9, %10 sc1\n\ts_waitcnt vmcnt(0)"
                   : "=&v"(q[0]), "=&v"(q[1]), "=&v"(q[2]), "=&v"(q[3]), "=&v"(q[4]), "=&v"(q[5]), "=&v"(q[6]), "=&v"(q[7])
                   : "v"(o1), "v"(o2), "s"(z), "s"(z + 4096), "s"(z + 8192), "s"(z + 12288) : "memory");
#pragma unroll
      for (int j = 0; j < 4; ++j) a[j] = cf{q[j].x - q[j].w, q[j].y + q[j].z};
#pragma unroll
      for (int j = 4; j < 8; ++j) a[j] = cf{q[j].x + q[j].w, q[j].z - q[j].y};
      if (lane == 0) { a[0] = cf{q[0].x, q[0].z}; a[4] = cf{q[0].y, q[0].w}; }
    }
    TS(9);
    fft512_wave<1>(a, wbuf, lane, twR1, twR2);
    asm volatile("" :: "v"(a[0]), "v"(a[7])); TS(10);
    const int kl = (lane >> 3) + 8 * (lane & 7);
    wave_sync();
    LAS u16* rows = (LAS u16*)wbuf;
#pragma unroll
    for (int k = 0; k < 8; ++k) {
      const unsigned p = pk_bf16(a[k].x, a[k].y);
      rows[kl + 64 * k] = (u16)p; rows[520 + kl + 64 * k] = (u16)(p >> 16);
    }
    wave_sync();
    {
      const int par = lane >> 5, nt = (lane >> 1) & 15, hh = lane & 1;
      const LAS u16* src = rows + par * 520 + nt * 32 + hh * 4;
      u32x2 q[4];
#pragma unroll
      for (int g = 0; g < 4; ++g) q[g] = *(const LAS u32x2*)(src + 8 * g);
      const char* bd = ws + WS_B + ((size_t)rbf * 1024 + (nb & 3) * 8 + w * 2) * 32;
      const unsigned bo = (unsigned)((nt * 64 + hh * 32 + par) * 32);
      st16_dev_s(bd, bo, u32x4{q[0][0], q[0][1], q[1][0], q[1][1]});
      st16_dev_s(bd + 16, bo, u32x4{q[2][0], q[2][1], q[3][0], q[3][1]});
    }
    TS(11);
    wait_vm<0>();
    TS(12);
    if (lds_ticket(SY(SY_PUB4), lane) == 3) {
      if (lane == 0) st8_dev(ws + WS_FL + 49152 + (xg * 4 + rm) * 256 + nb * 8, tag);
    }
  }
  TS(13);
  {
    const int fr_ = w >> 1, fh = w & 1;
    const char* og = ws + WS_O + (size_t)(rb0 + 8 * fr_) * 32768;
    const unsigned dst = (fr_ ? L_V : L_K) + fh * 14 * 1024 + lane * 16;
    u32x4 f[14];
    unsigned o[7];
#pragma unroll
    for (int i = 0; i < 7; ++i) o[i] = (unsigned)(((nb * 4 + 4 + fh * 14 + 2 * i) & 31) * 1024 + lane * 16);
    lds_wait_slow(SY(SY_ODONE), 2);
    lds_wait(SY(SY_ODONE), 4);
    TS(14);
    for (unsigned spin = 0; spin < SPIN_CAP; ++spin) {
#define LD2(i) asm volatile("global_load_dwordx4 %0, %2, %3 sc1\n\tglobal_load_dwordx4 %1, %2, %3 offset:1024 sc1" : "=&v"(f[2 * i]), "=&v"(f[2 * i + 1]) : "v"(o[i]), "s"(og) : "memory")
      LD2(0); LD2(1); LD2(2); LD2(3); LD2(4); LD2(5); LD2(6);
      asm volatile("s_waitcnt vmcnt(0)" : "+v"(f[0]), "+v"(f[1]), "+v"(f[2]), "+v"(f[3]), "+v"(f[4]), "+v"(f[5]), "+v"(f[6]), "+v"(f[7]), "+v"(f[8]), "+v"(f[9]),
                   "+v"(f[10]), "+v"(f[11]), "+v"(f[12]), "+v"(f[13]) :: "memory");
      unsigned m = 0u;
#pragma unroll
      for (int i = 0; i < 14; ++i) m = umax(m, umax(umax(f[i][0], f[i][1]), umax(f[i][2], f[i][3])));
      if (wave_all(m != SENT)) break;
      __builtin_amdgcn_s_sleep(2);
    }
#pragma unroll
    for (int i = 0; i < 14; ++i) *(LAS u32x4*)(lds + dst + i * 1024) = f[i];
    lds_signal(SY(SY_OSH + w), lane);
    TS(15);
    TDUMP(2);
  }
}

extern "C" void kernel_launch(void* const* d_in, const int* in_sizes, int n_in, void* d_out, int out_size, void* d_ws, size_t ws_size,
                              hipStream_t stream) {
  (void)hipFuncSetAttribute((const void*)k_main, hipFuncAttributeMaxDynamicSharedMemorySize, LDS_BYTES);
  k_main<<<256, 768, LDS_BYTES, stream>>>((const u16*)d_in[0], (const u16*)d_in[1], (const u16*)d_in[2], (const u16*)d_in[3], (const u16*)d_in[4],
                                          (const u16*)d_in[6], (const u16*)d_in[7], (const u16*)d_in[8], (char*)d_ws, (u16*)d_out);
}
```

```cpp
#include <hip/hip_runtime.h>
#include <hip/hip_bf16.h>
#include <stdint.h>

typedef short    bf16x8 __attribute__((ext_vector_type(8)));
typedef short    s16x4  __attribute__((ext_vector_type(4)));
typedef float    f32x16 __attribute__((ext_vector_type(16)));
typedef float    f32x4  __attribute__((ext_vector_type(4)));
typedef float    f32x2  __attribute__((ext_vector_type(2)));
typedef unsigned u32x4  __attribute__((ext_vector_type(4)));
typedef unsigned u32x2  __attribute__((ext_vector_type(2)));
typedef __bf16   bf16x2_t __attribute__((ext_vector_type(2)));
typedef f32x2 cf;
typedef unsigned short u16;
#define DI __device__ __forceinline__
#define LAS __attribute__((address_space(3)))
#define GAS __attribute__((address_space(1)))
#define MFMA32(a, b, c) __builtin_amdgcn_mfma_f32_32x32x16_bf16((a), (b), (c), 0, 0, 0)
#define MFMA16(a, b, c) __builtin_amdgcn_mfma_f32_16x16x32_bf16((a), (b), (c), 0, 0, 0)
#define SB() __builtin_amdgcn_sched_barrier(0)
#define MEMBAR() asm volatile("" ::: "memory")

constexpr int S = 2048, D = 512, DF = 257;

constexpr size_t WS_EP   = 0;
constexpr size_t WS_FL   = 4096;
constexpr size_t WS_K    = 4096 + 65536;
constexpr size_t WS_V    = WS_K + 65536;
constexpr size_t WS_KP   = WS_V + 65536;
constexpr size_t WS_G    = WS_KP + 4096;
constexpr size_t WS_PM   = WS_G + 8192;
constexpr size_t WS_X1   = WS_PM + 32768;
constexpr size_t WT_STRIDE = 8192 + 256;
constexpr size_t WS_WT   = WS_X1 + (size_t)256 * S * 8;
constexpr size_t Z_STRIDE = 16384 + 256;
constexpr size_t WS_Z    = WS_WT + 256 * WT_STRIDE;
constexpr size_t WS_B    = WS_Z + 256 * Z_STRIDE;
constexpr size_t WS_O    = WS_B + (size_t)S * D * 2;
constexpr size_t WS_END  = WS_O + (size_t)S * D * 2;
constexpr unsigned SENT = 0xFFFFFFFFu;

#define STAMP(k, slot) do {} while (0)
#define TS(i) do {} while (0)
#define TDECL do {} while (0)
#define TDUMP(k) do {} while (0)

DI unsigned pk_bf16(float lo, float hi) { f32x2 f = {lo, hi}; return __builtin_bit_cast(unsigned, __builtin_convertvector(f, bf16x2_t)); }
DI float bf_lo(unsigned u) { return __uint_as_float(u << 16); }
DI float bf_hi(unsigned u) { return __uint_as_float(u & 0xffff0000u); }
DI float bf_f(u16 u) { return __uint_as_float((unsigned)u << 16); }
DI float bfr(float x) { return bf_lo(pk_bf16(x, x)); }

DI void dma16x4(const void* sbase, unsigned v0, unsigned v1, unsigned v2, unsigned v3, unsigned lds_addr) {
  asm volatile("s_mov_b32 m0, %5\n\ts_nop 0\n\tglobal_load_lds_dwordx4 %0, %4\n\tglobal_load_lds_dwordx4 %1, %4 offset:1024\n\t"
               "global_load_lds_dwordx4 %2, %4 offset:2048\n\tglobal_load_lds_dwordx4 %3, %4 offset:3072"
               :: "v"(v0), "v"(v1), "v"(v2), "v"(v3), "s"(sbase), "s"(lds_addr) : "memory");
}
DI void dma16x4p(const void* sbase, unsigned v0, unsigned v1, unsigned v2, unsigned v3, unsigned lds_addr) {
  asm volatile("s_mov_b32 m0, %5\n\ts_nop 0\n\tglobal_load_lds_dwordx4 %0, %4\n\tglobal_load_lds_dwordx4 %1, %4 offset:1040\n\t"
               "global_load_lds_dwordx4 %2, %4 offset:2080\n\tglobal_load_lds_dwordx4 %3, %4 offset:3120"
               :: "v"(v0), "v"(v1), "v"(v2), "v"(v3), "s"(sbase), "s"(lds_addr) : "memory");
}
template <int N> DI void wait_vm() { asm volatile("s_waitcnt vmcnt(%0)" :: "i"(N) : "memory"); }
DI void lds_signal(unsigned addr, int) {
  asm volatile("s_waitcnt lgkmcnt(0)\n\ts_mov_b64 exec, 1\n\tds_add_u32 %0, %1\n\ts_mov_b64 exec, -1" :: "v"(addr), "v"(1u) : "memory");
}
DI void lds_wait(unsigned addr, unsigned n) {
  MEMBAR();
  while ((unsigned)__builtin_amdgcn_readfirstlane(__hip_atomic_load((LAS unsigned*)addr, __ATOMIC_RELAXED, __HIP_MEMORY_SCOPE_WORKGROUP)) < n) __builtin_amdgcn_s_sleep(1);
  MEMBAR();
}
DI void lds_wait_slow(unsigned addr, unsigned n) {
  MEMBAR();
  while ((unsigned)__builtin_amdgcn_readfirstlane(__hip_atomic_load((LAS unsigned*)addr, __ATOMIC_RELAXED, __HIP_MEMORY_SCOPE_WORKGROUP)) < n) __builtin_amdgcn_s_sleep(6);
  MEMBAR();
}
DI void wave_sync() { __builtin_amdgcn_fence(__ATOMIC_RELEASE, "wavefront"); __builtin_amdgcn_wave_barrier(); __builtin_amdgcn_fence(__ATOMIC_ACQUIRE, "wavefront"); }
DI void st16_dev(void* p, u32x4 v) { asm volatile("global_store_dwordx4 %0, %1, off sc1\n\ts_nop 1" :: "v"(p), "v"(v) : "memory"); }

DI void st8_dev(void* p, u32x2 v) { asm volatile("global_store_dwordx2 %0, %1, off sc1" :: "v"(p), "v"(v) : "memory"); }
DI void st4_dev(void* p, unsigned v) { asm volatile("global_store_dword %0, %1, off sc1" :: "v"(p), "v"(v) : "memory"); }
#define SC1S " sc1"
DI void dma16x4_dev(const void* sbase, unsigned v0, unsigned lds_addr) {
  asm volatile("s_mov_b32 m0, %2\n\ts_nop 0\n\tglobal_load_lds_dwordx4 %0, %1" SC1S "\n\tglobal_load_lds_dwordx4 %0, %1 offset:1024" SC1S "\n\t"
               "global_load_lds_dwordx4 %0, %1 offset:2048" SC1S "\n\tglobal_load_lds_dwordx4 %0, %1 offset:3072" SC1S
               :: "v"(v0), "s"(sbase), "s"(lds_addr) : "memory");
}
DI unsigned lds_ticket(unsigned addr, int) {
  unsigned t;
  asm volatile("s_waitcnt lgkmcnt(0)\n\ts_mov_b64 exec, 1\n\tds_add_rtn_u32 %0, %1, %2\n\ts_mov_b64 exec, -1\n\ts_waitcnt lgkmcnt(0)" : "=&v"(t) : "v"(addr), "v"(1u) : "memory");
  return __builtin_amdgcn_readfirstlane(t);
}
DI void st16_dev_s(const void* sbase, unsigned voff, u32x4 v) { asm volatile("global_store_dwordx4 %0, %1, %2 sc1\n\ts_nop 1" :: "v"(voff), "v"(v), "s"(sbase) : "memory"); }
template <int CTRL> DI float dppf(float x) { return __int_as_float(__builtin_amdgcn_update_dpp(0, __float_as_int(x), CTRL, 0xf, 0xf, true)); }
DI float x16_sum(float x) { auto r = __builtin_amdgcn_permlane16_swap(__float_as_uint(x), __float_as_uint(x), false, false); return __uint_as_float(r[0]) + __uint_as_float(r[1]); }
DI float x32_sum(float x) { auto r = __builtin_amdgcn_permlane32_swap(__float_as_uint(x), __float_as_uint(x), false, false); return __uint_as_float(r[0]) + __uint_as_float(r[1]); }
DI float x16_max(float x) { auto r = __builtin_amdgcn_permlane16_swap(__float_as_uint(x), __float_as_uint(x), false, false); return fmaxf(__uint_as_float(r[0]), __uint_as_float(r[1])); }
DI float x32_max(float x) { auto r = __builtin_amdgcn_permlane32_swap(__float_as_uint(x), __float_as_uint(x), false, false); return fmaxf(__uint_as_float(r[0]), __uint_as_float(r[1])); }
DI float row16_sum(float x) { x += dppf<0xB1>(x); x += dppf<0x4E>(x); x += dppf<0x141>(x); x += dppf<0x140>(x); return x; }
DI float row16_max(float x) { x = fmaxf(x, dppf<0xB1>(x)); x = fmaxf(x, dppf<0x4E>(x)); x = fmaxf(x, dppf<0x141>(x)); x = fmaxf(x, dppf<0x140>(x)); return x; }
DI float wave_sum(float x) { return x32_sum(x16_sum(row16_sum(x))); }
DI float wave_max(float x) { return x32_max(x16_max(row16_max(x))); }
DI unsigned umax(unsigned a, unsigned b) { return a > b ? a : b; }
DI bool wave_all(bool p) { return __builtin_amdgcn_ballot_w64(!p) == 0ull; }

DI cf cmul(cf a, cf w)  { cf t = a.xx * w; cf u = {-w.y, w.x}; return a.yy * u + t; }
DI cf cmulc(cf a, cf w) { cf t = a * w.xx; cf u = {w.y, -w.y}; return a.yx * u + t; }
template <int INV> DI cf rot90(cf a) { return INV ? cf{-a.y, a.x} : cf{a.y, -a.x}; }
template <int INV> DI cf ctw(cf a, cf w) { return INV ? cmulc(a, w) : cmul(a, w); }
template <int INV> DI void dft4(cf& c0, cf& c1, cf& c2, cf& c3) {
  cf d0 = c0 + c2, d2 = c0 - c2, d1 = c1 + c3, d3 = rot90<INV>(c1 - c3);
  c0 = d0 + d1; c2 = d0 - d1; c1 = d2 + d3; c3 = d2 - d3;
}
template <int INV> DI void dft8(cf (&a)[8]) {
  constexpr float R = 0.70710678118654752440f;
  cf b0 = a[0] + a[4], b4 = a[0] - a[4], b1 = a[1] + a[5], b5 = a[1] - a[5];
  cf b2 = a[2] + a[6], b6 = a[2] - a[6], b3 = a[3] + a[7], b7 = a[3] - a[7];
  cf r5 = rot90<INV>(b5), r7 = rot90<INV>(b7);
  b5 = (b5 + r5) * R;  b6 = rot90<INV>(b6);  b7 = (r7 - b7) * R;
  dft4<INV>(b0, b1, b2, b3);
  dft4<INV>(b4, b5, b6, b7);
  a[0] = b0; a[2] = b1; a[4] = b2; a[6] = b3;
  a[1] = b4; a[3] = b5; a[5] = b6; a[7] = b7;
}
DI cf twid(float rev) { return cf{__builtin_amdgcn_cosf(rev), -__builtin_amdgcn_sinf(rev)}; }
DI cf csq(cf a) { return cf{a.x * a.x - a.y * a.y, 2.f * a.x * a.y}; }
DI void twid7(cf (&w)[8], float rev) {
  w[1] = twid(rev); w[2] = csq(w[1]); w[3] = cmul(w[2], w[1]); w[4] = csq(w[2]); w[5] = cmul(w[4], w[1]); w[6] = csq(w[3]); w[7] = cmul(w[4], w[3]);
}

template <int INV>
DI void fft512_wave(cf (&a)[8], LAS cf* buf  , int lane, const cf (&w1)[8], const cf (&w2)[8]) {
  const int k1 = lane >> 3, n3 = lane & 7;
  dft8<INV>(a);
#pragma unroll
  for (int k = 1; k < 8; ++k) a[k] = ctw<INV>(a[k], w1[k]);
#pragma unroll
  for (int k = 0; k < 8; ++k) buf[k * 72 + lane] = a[k];
  wave_sync();
#pragma unroll
  for (int j = 0; j < 8; ++j) a[j] = buf[k1 * 72 + n3 + 8 * j];
  dft8<INV>(a);
#pragma unroll
  for (int k = 1; k < 8; ++k) a[k] = ctw<INV>(a[k], w2[k]);
  wave_sync();
#pragma unroll
  for (int k = 0; k < 8; ++k) buf[k1 * 72 + 9 * k + n3] = a[k];
  wave_sync();
#pragma unroll
  for (int j = 0; j < 8; ++j) a[j] = buf[k1 * 72 + 9 * n3 + j];
  dft8<INV>(a);
}
DI void pin8(cf (&w)[8]) { asm volatile("" : "+v"(w[1]), "+v"(w[2]), "+v"(w[3]), "+v"(w[4]), "+v"(w[5]), "+v"(w[6]), "+v"(w[7])); }

constexpr unsigned L_X = 0  , L_W = 66560  , L_K = 0, L_V = 65536,
                   L_ONES = 132096, L_ZERO = L_ONES + 2048, L_FFT = L_ZERO + 2048, L_QS = L_FFT + 18688  ,
                   L_SYNC = L_QS + 8192, LDS_BYTES = L_SYNC + 256;
enum { SY_W0 = 0, SY_W1, SY_W2, SY_W3, SY_X, SY_KVPUB, SY_GO1, SY_XWFREE, SY_QS, SY_F1A, SY_F1B, SY_PUB2, SY_GO2, SY_KV0, SY_KV1, SY_FB, SY_PUB3, SY_GO3,
       SY_FFTDONE, SY_PUB4, SY_GO4, SY_PARKED, SY_PARK0, SY_PARK1, SY_PARK2, SY_PARK3, SY_ODONE, SY_OSH, SY_TAGOK, SY_XISS, SY_KV2, SY_TAG = 32   };
#define SY(i) (lb + L_SYNC + (i) * 4)
#define XDELAY lds_wait(SY(SY_KV1), 4)
#define F1PRIO 3
#define GPRIO 1
#define WDELAY lds_wait(SY(SY_XISS), 4)
constexpr unsigned SPIN_CAP = 1u << 16;

#define EX(i) Sc[i] = __builtin_amdgcn_exp2f(Sc[i]); SB()
#define CV(P, j, i) P[j] = pk_bf16(Sc[i], Sc[i + 1]); SB()
DI void att_step(f32x16& Sc, f32x16& Sn, f32x16& Op, f32x16& Oc, const f32x16& C, const bf16x8& qn, const bf16x8& kn, const bf16x8& v1p, const bf16x8& v0c,
                 u32x4& p1) {
  u32x4 p0;
  Sn = MFMA32(kn, qn, C); SB();
  EX(0); EX(1); EX(2); EX(3); CV(p0, 0, 0); EX(4); CV(p0, 1, 2); EX(5);
  Op = MFMA32(v1p, __builtin_bit_cast(bf16x8, p1), Op); SB();
  EX(6); EX(7); CV(p0, 2, 4); EX(8); CV(p0, 3, 6); EX(9); EX(10);
  Oc = MFMA32(v0c, __builtin_bit_cast(bf16x8, p0), Oc); SB();
  EX(11); CV(p1, 0, 8); EX(12); EX(13); CV(p1, 1, 10); EX(14); EX(15); CV(p1, 2, 12); CV(p1, 3, 14);
}
DI void poll_all(const char* fl, u32x2 tag, int lane) {
  for (unsigned spin = 0; spin < SPIN_CAP; ++spin) {
    u32x4 a, c;
    asm volatile("global_load_dwordx4 %0, %2, %3 sc1\n\tglobal_load_dwordx4 %1, %2, %3 offset:1024 sc1\n\ts_waitcnt vmcnt(0)"
                 : "=&v"(a), "=&v"(c) : "v"(lane * 16), "s"(fl) : "memory");
    const unsigned d = ((a[0] ^ tag[0]) | (a[2] ^ tag[0]) | (c[0] ^ tag[0]) | (c[2] ^ tag[0])) | ((a[1] ^ tag[1]) | (a[3] ^ tag[1]) | (c[1] ^ tag[1]) | (c[3] ^ tag[1]));
    if (wave_all(d == 0u)) break;
    __builtin_amdgcn_s_sleep(2);
  }
}

__attribute__((target("no-packed-fp32-ops"))) __global__ __launch_bounds__(768, 3)
void k_main(const u16* __restrict__ x, const u16* __restrict__ wq, const u16* __restrict__ wk, const u16* __restrict__ wv, const u16* __restrict__ wo,
            const u16* __restrict__ w_r, const u16* __restrict__ w_i, const u16* __restrict__ gw, char* __restrict__ ws, u16* __restrict__ out) {
  extern __shared__ __attribute__((aligned(16))) char lds_[];
  LAS char* lds = (LAS char*)lds_;
  const unsigned lb = (unsigned)(uintptr_t)lds;
  const int tid = threadIdx.x, wid = __builtin_amdgcn_readfirstlane(tid >> 6), lane = tid & 63, b = blockIdx.x;
  const int xg = b & 7, t0 = xg * 32 + (b >> 3), rm = b >> 6, nb = (b >> 3) & 7;
  const int rb0 = xg + 16 * rm;
  const int r = lane & 31, h = lane >> 5;
  TDECL;
  TS(0);
  if (tid < 64) *(LAS unsigned*)(lds + L_SYNC + tid * 4) = 0u;
  else if (tid >= 256) { const unsigned c = tid < 512 ? 0x3f803f80u : 0u; *(LAS u32x2*)(lds + L_ONES + (tid - 256) * 8) = u32x2{c, c}; }
  asm volatile("s_waitcnt lgkmcnt(0)" ::: "memory");
  __builtin_amdgcn_s_barrier();
  MEMBAR();

  if (wid < 8) {
    const int pr = wid & 3, wr = pr >> 1, hp = pr & 1, kh = wid >> 2;
    bf16x8 qA, qB;
    if (kh == 0) {
      {
        unsigned vw[4];
#pragma unroll
        for (int i = 0; i < 4; ++i) vw[i] = (unsigned)((7 * i + (lane >> 3)) * 1024 + (((lane & 7) ^ (((lane >> 4) & 1) << 2)) << 4));
        const char* wb = (const char*)wq + (size_t)wid * 32768 + nb * 128;
        WDELAY;
#pragma unroll
        for (int c = 0; c < 4; ++c) dma16x4(wb + c * 131072, vw[0], vw[1], vw[2], vw[3], lb + L_W + c * 16384 + wid * 4096);
      }
      const int g = lane >> 4, q = (lane & 15) >> 2, p = lane & 3;
      LAS char* wbase = lds + L_W + (8 * h + q) * 128 + ((hp ^ (q >> 1)) * 64) + 32 * (g & 1) + 8 * p;
      LAS char* xbase = lds + L_X + (32 * wr + r) * 1040 + h * 16;
      f32x16 acc = {};
      __builtin_amdgcn_s_setprio(GPRIO);
      wait_vm<8>();  lds_signal(SY(SY_W0), lane);
      wait_vm<0>();  lds_signal(SY(SY_W1), lane);
      lds_wait(SY(SY_X), 4);
      bf16x8 xf[2][4]; s16x4 lo[2][4], hi[2][4];
      auto fetch = [&](int hc, int s_) __attribute__((always_inline)) {
        if ((hc & 3) == 0) lds_wait(SY(SY_W0 + (hc >> 2)), 4);
#pragma unroll
        for (int t = 0; t < 4; ++t) {
          xf[s_][t] = *(LAS bf16x8*)(xbase + (hc * 4 + t) * 32);
          lo[s_][t] = __builtin_amdgcn_ds_read_tr16_b64_v4i16((LAS s16x4*)(wbase + (hc * 4 + t) * 2048));
          hi[s_][t] = __builtin_amdgcn_ds_read_tr16_b64_v4i16((LAS s16x4*)(wbase + (hc * 4 + t) * 2048 + 512));
        }
        SB();
      };
      TS(1);
      fetch(0, 0);
#pragma unroll
      for (int hc = 0; hc < 8; ++hc) {
        if (hc < 7) fetch(hc + 1, (hc + 1) & 1);
#pragma unroll
        for (int t = 0; t < 4; ++t) acc = MFMA32(__builtin_shufflevector(lo[hc & 1][t], hi[hc & 1][t], 0, 1, 2, 3, 4, 5, 6, 7), xf[hc & 1][t], acc);
        SB();
      }
      lds_signal(SY(SY_XWFREE), lane);
      constexpr float C = 0.25f * 1.4426950408889634f;
      u32x4 oa, ob;
#pragma unroll
      for (int i = 0; i < 4; ++i) { oa[i] = pk_bf16(acc[2 * i] * C, acc[2 * i + 1] * C); ob[i] = pk_bf16(acc[8 + 2 * i] * C, acc[9 + 2 * i] * C); }
      qA = __builtin_bit_cast(bf16x8, oa); qB = __builtin_bit_cast(bf16x8, ob);
      lds_wait(SY(SY_KVPUB), 4);
      *(LAS u32x4*)(lds + L_QS + pr * 2048 + lane * 16) = oa;
      *(LAS u32x4*)(lds + L_QS + pr * 2048 + 1024 + lane * 16) = ob;
      lds_signal(SY(SY_QS), lane);
      __builtin_amdgcn_s_setprio(0);
      TS(2);
    } else {
      u32x2 ep;
      __builtin_amdgcn_s_setprio(3);
      {
        const unsigned v0 = 64 + lane * 16;
        const char* xb = (const char*)x + (size_t)((rb0 + 8 * wr) * 32 + hp * 16) * 1024 - 64;
#pragma unroll
        for (int gi = 0; gi < 4; ++gi) dma16x4p(xb + gi * 4096, v0, v0 - 16, v0 - 32, v0 - 48, lb + L_X + (pr * 16 + gi * 4) * 1040);
        u32x2 kv0, kv1;
        const u16* kvs = (nb < 4 ? wk : wv) + 4 * (nb & 3) + pr * 2048;
        asm volatile("global_load_dwordx2 %0, %3, %4\n\tglobal_load_dwordx2 %1, %3, %4 offset:2048\n\tglobal_load_dwordx2 %2, %5, %6 sc1"
                     : "=&v"(kv0), "=&v"(kv1), "=&v"(ep) : "v"(lane * 32), "s"(kvs), "v"(0), "s"(ws + WS_EP) : "memory");
        lds_signal(SY(SY_XISS), lane);
        asm volatile("s_waitcnt vmcnt(0)" : "+v"(kv0), "+v"(kv1), "+v"(ep) :: "memory");
        *(LAS u32x2*)(lds + L_QS + pr * 1024 + lane * 8) = kv0;
        *(LAS u32x2*)(lds + L_QS + pr * 1024 + 512 + lane * 8) = kv1;
        lds_signal(SY(SY_X), lane);
      }
      const u32x4 gq = *(const u32x4*)(gw + lane * 8);
      lds_wait(SY(SY_X), 4);
      TS(1);
      {
        const int g = lane >> 4, q = (lane & 15) >> 2, row2 = 16 * pr + (lane & 15);
        LAS char* ab = lds + L_QS + (8 * g + q) * 8;
        LAS char* xb2 = lds + L_X + row2 * 1040 + g * 16;
        f32x4 acc2 = {};
        const int tile = rb0 + 8 * wr;
        bf16x8 yf[16], af[16];
#pragma unroll
        for (int t = 0; t < 16; ++t) {
          yf[t] = *(LAS bf16x8*)(xb2 + t * 64);
          const s16x4 l2 = __builtin_amdgcn_ds_read_tr16_b64_v4i16((LAS s16x4*)(ab + t * 256)), h2 = __builtin_amdgcn_ds_read_tr16_b64_v4i16((LAS s16x4*)(ab + t * 256 + 32));
          af[t] = __builtin_shufflevector(l2, h2, 0, 1, 2, 3, 4, 5, 6, 7);
        }
        SB();
        if (nb < 4) {
#pragma unroll
          for (int t = 0; t < 16; ++t) acc2 = MFMA16(af[t], yf[t], acc2);
          const unsigned p0 = pk_bf16(acc2[0], acc2[1]), p1 = pk_bf16(acc2[2], acc2[3]);
          if (lane < 16) st8_dev(ws + WS_K + tile * 1024 + (nb & 1) * 512 + (row2 & 31) * 16 + (nb >> 1) * 8, u32x2{p0, p1});
          float n2 = bf_lo(p0) * bf_lo(p0) + bf_hi(p0) * bf_hi(p0) + bf_lo(p1) * bf_lo(p1) + bf_hi(p1) * bf_hi(p1);
          n2 = row16_max(n2);
          if (lane == 0) st4_dev(ws + WS_KP + ((tile * 2 + hp) * 4 + nb) * 4, __builtin_bit_cast(unsigned, n2));
        } else {
#pragma unroll
          for (int t = 0; t < 16; ++t) acc2 = MFMA16(yf[t], af[t], acc2);
          if ((lane & 15) < 4)
            st8_dev(ws + WS_V + tile * 1024 + hp * 512 + (g & 1) * 256 + ((nb - 4) * 4 + (lane & 15)) * 16 + (g >> 1) * 8, u32x2{pk_bf16(acc2[0], acc2[1]), pk_bf16(acc2[2], acc2[3])});
        }
      }
      const int r0 = 8 * nb + 2 * pr;
      const u32x4 xa = *(LAS u32x4*)(lds + L_X + r0 * 1040 + lane * 16);
      const u32x4 xb = *(LAS u32x4*)(lds + L_X + (r0 + 1) * 1040 + lane * 16);
      wait_vm<0>();
      lds_signal(SY(SY_XWFREE), lane);
      const unsigned tk = lds_ticket(SY(SY_KVPUB), lane);
      TS(2);
      u32x2 tag;
      { const unsigned long long e = (((unsigned long long)ep[1] << 32) | ep[0]) + 1ull; tag = u32x2{(unsigned)e, (unsigned)(e >> 32)}; }
      if (tk == 3) {
        if (lane < 8) st8_dev(ws + WS_FL + lane * 2048 + b * 8, tag);
        if (lane == 0) *(LAS u32x2*)(lds + L_SYNC + SY_TAG * 4) = tag;
        lds_signal(SY(SY_TAGOK), lane);
      }
      {
        float gs1 = 0.f, gs2 = 0.f;
#pragma unroll
        for (int i = 0; i < 4; ++i) {
          const unsigned pa = pk_bf16(bf_lo(xa[i]) * bf_lo(gq[i]), bf_hi(xa[i]) * bf_hi(gq[i])), pb = pk_bf16(bf_lo(xb[i]) * bf_lo(gq[i]), bf_hi(xb[i]) * bf_hi(gq[i]));
          gs1 += bf_lo(pa) + bf_hi(pa); gs2 += bf_lo(pb) + bf_hi(pb);
        }
        gs1 = wave_sum(gs1); gs2 = wave_sum(gs2);
        if (lane < 2) {
          const float z = bfr(lane ? gs2 : gs1);
          st4_dev(ws + WS_G + ((rb0 + 8 * (r0 >> 5)) * 32 + (r0 & 31) + lane) * 4,
                  __builtin_bit_cast(unsigned, bfr(__builtin_amdgcn_rcpf(1.0f + __builtin_amdgcn_exp2f(z * -1.4426950408889634f)))));
        }
      }
      wait_vm<0>();
      lds_signal(SY(SY_PUB2), lane);
      if (tk == 3) {
        poll_all(ws + WS_FL + xg * 2048, tag, lane);
        lds_signal(SY(SY_GO1), lane);
        if (b == 0 && lane == 0) st8_dev(ws + WS_EP, tag);
      }
      __builtin_amdgcn_s_setprio(0);
      lds_wait(SY(SY_QS), 4);
      qA = *(LAS bf16x8*)(lds + L_QS + pr * 2048 + lane * 16);
      qB = *(LAS bf16x8*)(lds + L_QS + pr * 2048 + 1024 + lane * 16);
    }
    float qn2;
    {
      const u32x4 qu = __builtin_bit_cast(u32x4, qA), qv = __builtin_bit_cast(u32x4, qB);
      float na = 0.f, nq = 0.f;
#pragma unroll
      for (int i = 0; i < 4; ++i) { float a = bf_lo(qu[i]), c = bf_hi(qu[i]); na += a * a + c * c; a = bf_lo(qv[i]); c = bf_hi(qv[i]); nq += a * a + c * c; }
      qn2 = fmaxf(x32_sum(na), x32_sum(nq));
    }
    lds_wait(SY(SY_XWFREE), 8);
    lds_wait(SY(SY_GO1), 1);
    TS(3);
    f32x4 kp0, kp1;
    asm volatile("global_load_dwordx4 %0, %2, %3 sc1\n\tglobal_load_dwordx4 %1, %2, %3 offset:1024 sc1" : "=&v"(kp0), "=&v"(kp1) : "v"(lane * 16), "s"(ws + WS_KP) : "memory");
    {
      const int off = (pr * 32 + kh * 2) * 1024;
      asm volatile("s_mov_b32 m0, %2\n\ts_nop 0\n\tglobal_load_lds_dwordx4 %0, %1 sc1\n\tglobal_load_lds_dwordx4 %0, %1 offset:1024 sc1"
                   :: "v"(lane * 16), "s"(ws + WS_K + off), "s"(lb + off) : "memory");
    }
    asm volatile("s_waitcnt vmcnt(2)" : "+v"(kp0), "+v"(kp1) :: "memory");
    const float km = wave_max(fmaxf(kp0[0] + kp0[1] + kp0[2] + kp0[3], kp1[0] + kp1[1] + kp1[2] + kp1[3]));
    const float negm = -__builtin_amdgcn_sqrtf(qn2 * km) * 1.0001f;
    LAS char* kp = lds + L_K + lane * 16;
    const int vstep = (r < 16) ? 2048 : 0;
    const bool onesrow = (r & 27) == 16;
    f32x16 OA = {}, OB = {};
    {
      f32x16 C;
#pragma unroll
      for (int i = 0; i < 16; ++i) C[i] = negm;
      int va = (r < 16) ? (L_V + kh * 32768 + h * 256 + r * 16) : ((onesrow ? L_ONES : L_ZERO) + h * 256 + (r - 16) * 16);
      int ka = kh * 32768;
      wait_vm<0>();
      lds_signal(SY(SY_KV0), lane);
      lds_wait(SY(SY_KV0), 8);
      if (kh == 1) {
#pragma unroll
        for (int i = 0; i < 7; ++i) { const int off = (pr * 32 + 4 + 4 * i) * 1024; dma16x4_dev(ws + WS_K + off, lane * 16, lb + off); }
        wait_vm<20>(); lds_signal(SY(SY_KV1), lane);
        wait_vm<0>();  lds_signal(SY(SY_KV2), lane);
      }
      TS(4);
#define LDK(o) (*(LAS bf16x8*)(kp + ka + (o)))
#define LDV(o) (*(LAS bf16x8*)(lds + va + (o)))
      bf16x8 k0 = LDK(0), k1, v00 = LDV(0), v10 = LDV(512), v01, v11 = v10;
      f32x16 Sa = MFMA32(k0, qA, C), Sb;
      u32x4 p1 = {0u, 0u, 0u, 0u};
#pragma unroll 1
      for (int ph = 0; ph < 3; ++ph) {
        const int nit = ph == 0 ? 1 : (ph == 1 ? 4 : 11);
#pragma unroll 1
        for (int it = 0; it < nit; ++it) {
          k1 = LDK(1024); v01 = LDV(1024); SB();
          att_step(Sa, Sb, OB, OA, C, qB, k0, v11, v00, p1);
          v11 = LDV(1024 + 512); SB();
          att_step(Sb, Sa, OA, OB, C, qA, k1, v10, v00, p1);
          va += vstep;
          k0 = LDK(2048); v00 = LDV(0); SB();
          att_step(Sa, Sb, OB, OA, C, qB, k1, v10, v01, p1);
          v10 = LDV(512); SB();
          att_step(Sb, Sa, OA, OB, C, qA, k0, v11, v01, p1);
          ka += 2048;
        }
        if (ph < 2) lds_wait(SY(SY_KV1) + ph * (SY_KV2 - SY_KV1) * 4, 4);
      }
      OB = MFMA32(v11, __builtin_bit_cast(bf16x8, p1), OB);
    }
    TS(5);
    LAS float* red = (LAS float*)(lds + L_K + pr * 4608) + lane;
    const int rbw = rb0 + 8 * wr;
    if (kh == 0) {
      lds_signal(SY(SY_PARKED), lane);
      lds_wait(SY(SY_PARKED), 4);
      {
        unsigned vw[4];
#pragma unroll
        for (int i = 0; i < 4; ++i) vw[i] = (unsigned)((7 * i + (lane >> 3)) * 1024 + (((lane & 7) ^ (((lane >> 4) & 1) << 2)) << 4));
        const char* wb = (const char*)wo + (size_t)pr * 131072 + nb * 128;
        const unsigned wd = lb + (wr ? L_V : L_K) + hp * 16384;
#pragma unroll
        for (int c = 0; c < 4; ++c) dma16x4(wb + c * 32768, vw[0], vw[1], vw[2], vw[3], wd + c * 4096);
        wait_vm<0>();
        lds_signal(SY(SY_PARKED), lane);
        lds_wait(SY(SY_PARKED), 8);
      }
      bf16x8 wf[32];
      {
        const int g = lane >> 4, q = (lane & 15) >> 2, p = lane & 3;
        const unsigned wl = (unsigned)((4 * h + q) * 128 + ((hp ^ (q >> 1)) * 64) + 32 * (g & 1) + 8 * p);
#pragma unroll
        for (int k4 = 0; k4 < 8; ++k4) {
          const int hd = (nb + k4) & 7;
          LAS char* src = lds + wl + (hd >> 2) * L_V + (hd & 3) * 8192;
#pragma unroll
          for (int e = 0; e < 4; ++e) {
            const s16x4 lo = __builtin_amdgcn_ds_read_tr16_b64_v4i16((LAS s16x4*)(src + e * 2048)), hi = __builtin_amdgcn_ds_read_tr16_b64_v4i16((LAS s16x4*)(src + e * 2048 + 1024));
            wf[4 * k4 + e] = __builtin_shufflevector(lo, hi, 0, 1, 2, 3, 4, 5, 6, 7);
          }
        }
      }
      lds_signal(SY(SY_PARKED), lane);
      lds_wait(SY(SY_PARKED), 12);
#pragma unroll
      for (int i = 0; i < 9; ++i) { red[i * 64] = OA[i]; red[(9 + i) * 64] = OB[i]; }
      lds_signal(SY(SY_PARK0 + pr), lane);
      TS(8);
      const int nt = nb * 2 + hp;
      lds_wait_slow(SY(SY_GO4), 1);
      u32x4 b0, b1; float g;
      asm volatile("global_load_dwordx4 %0, %3, %4 sc1\n\tglobal_load_dwordx4 %1, %3, %4 offset:16 sc1\n\tglobal_load_dword %2, %5, %6 sc1\n\ts_waitcnt vmcnt(0)"
                   : "=&v"(b0), "=&v"(b1), "=&v"(g) : "v"(lane * 32), "s"(ws + WS_B + ((size_t)rbw * 16 + nt) * 2048), "v"(r * 4), "s"(ws + WS_G + rbw * 128) : "memory");
      {
        const float omg = bfr(1.0f - g);
#pragma unroll
        for (int i = 0; i < 4; ++i) { b0[i] = pk_bf16(omg * bf_lo(b0[i]), omg * bf_hi(b0[i])); b1[i] = pk_bf16(omg * bf_lo(b1[i]), omg * bf_hi(b1[i])); }
      }
      TS(9);
      lds_wait_slow(SY(SY_ODONE), 2);
      lds_wait(SY(SY_ODONE), 4);
      f32x16 acc = {};
#pragma unroll
      for (int k = 0; k < 4; ++k) acc = MFMA32(wf[k], *(LAS bf16x8*)(lds + L_QS + (wr * 4 + k) * 1024 + lane * 16), acc);
      lds_wait(SY(SY_OSH), 4);
      TS(10);
      {
        LAS char* of = lds + (wr ? L_V : L_K) + lane * 16;
#pragma unroll
        for (int k = 4; k < 32; ++k) acc = MFMA32(wf[k], *(LAS bf16x8*)(of + (k - 4) * 1024), acc);
      }
      u16* op = out + (size_t)(rbw * 32 + r) * D + nt * 32 + 4 * h;
#pragma unroll
      for (int q = 0; q < 4; ++q) {
        const unsigned t01 = q < 2 ? b0[2 * q] : b1[2 * q - 4], t23 = q < 2 ? b0[2 * q + 1] : b1[2 * q - 3];
        const unsigned a01 = pk_bf16(acc[4 * q], acc[4 * q + 1]), a23 = pk_bf16(acc[4 * q + 2], acc[4 * q + 3]);
        const unsigned g01 = pk_bf16(g * bf_lo(a01), g * bf_hi(a01)), g23 = pk_bf16(g * bf_lo(a23), g * bf_hi(a23));
        *(u32x2*)(op + 8 * q) = u32x2{pk_bf16(bf_lo(g01) + bf_lo(t01), bf_hi(g01) + bf_hi(t01)), pk_bf16(bf_lo(g23) + bf_lo(t23), bf_hi(g23) + bf_hi(t23))};
      }
      TS(6);
      TDUMP(0);
      return;
    }
    lds_wait(SY(SY_PUB3), 4);
    lds_wait(SY(SY_PARK0 + pr), 1);
#pragma unroll
    for (int i = 0; i < 9; ++i) { OA[i] += red[i * 64]; OB[i] += red[(9 + i) * 64]; }
    if (!wave_all(OA[8] >= 1.2621774483536189e-29f && OB[8] >= 1.2621774483536189e-29f)) {
      __builtin_amdgcn_fence(__ATOMIC_ACQUIRE, "agent");
      const bf16x8* kg = (const bf16x8*)(ws + WS_K) + lane;
      const bf16x8* vg = (const bf16x8*)(ws + WS_V + h * 256 + (r & 15) * 16);
      const short cv = onesrow ? (short)0x3f80 : (short)0;
      const bf16x8 vc = {cv, cv, cv, cv, cv, cv, cv, cv};
#pragma unroll 1
      for (int hh = 0; hh < 2; ++hh) {
        const bf16x8 qf = hh ? qB : qA;
        float m = -__builtin_inff();
        f32x16 C = {};
#pragma unroll 1
        for (int t = 0; t < 64; ++t) {
          f32x16 Sx = MFMA32(kg[t * 64], qf, C);
#pragma unroll
          for (int i = 0; i < 16; ++i) m = fmaxf(m, Sx[i]);
        }
        m = x32_max(m);
#pragma unroll
        for (int i = 0; i < 16; ++i) C[i] = -m;
        f32x16 O = {};
#pragma unroll 1
        for (int t = 0; t < 64; ++t) {
          f32x16 Sx = MFMA32(kg[t * 64], qf, C);
          const bf16x8 va0 = vg[t * 64], va1 = vg[t * 64 + 32];
          u32x4 p0, p1;
#pragma unroll
          for (int i = 0; i < 4; ++i) {
            p0[i] = pk_bf16(__builtin_amdgcn_exp2f(Sx[2 * i]), __builtin_amdgcn_exp2f(Sx[2 * i + 1]));
            p1[i] = pk_bf16(__builtin_amdgcn_exp2f(Sx[8 + 2 * i]), __builtin_amdgcn_exp2f(Sx[9 + 2 * i]));
          }
          O = MFMA32(r < 16 ? va0 : vc, __builtin_bit_cast(bf16x8, p0), O);
          O = MFMA32(r < 16 ? va1 : vc, __builtin_bit_cast(bf16x8, p1), O);
        }
        if (hh) OB = O; else OA = O;
      }
    }
    {
      const float ia = __builtin_amdgcn_rcpf(OA[8]), ib = __builtin_amdgcn_rcpf(OB[8]);
      u32x4 fa, fb;
#pragma unroll
      for (int i = 0; i < 4; ++i) {
        fa[i] = pk_bf16(OA[2 * i] * ia, OA[2 * i + 1] * ia); fb[i] = pk_bf16(OB[2 * i] * ib, OB[2 * i + 1] * ib);
        if (fa[i] == SENT) fa[i] = 0x7fc07fc0u;
        if (fb[i] == SENT) fb[i] = 0x7fc07fc0u;
      }
      char* og = ws + WS_O + ((size_t)(rbw * 32 + nb * 4 + hp * 2) * 64 + lane) * 16;
      st16_dev(og, fa); st16_dev(og + 1024, fb);
      *(LAS u32x4*)(lds + L_QS + pr * 2048 + lane * 16) = fa;
      *(LAS u32x4*)(lds + L_QS + pr * 2048 + 1024 + lane * 16) = fb;
      lds_signal(SY(SY_ODONE), lane);
    }
    TS(6);
    TDUMP(1);
    return;
  }

  __builtin_amdgcn_s_setprio(F1PRIO);
  const int w = wid - 8, t = tid - 512;
  LAS cf* buf = (LAS cf*)(lds + L_FFT);
  LAS cf* wbuf = buf + w * 576;
  const int k1n = lane & 7, in_ = (lane >> 3) + 8 * w;
  const int k1 = t >> 5, k2 = (t >> 2) & 7, ip = t & 3;
  cf twR1[8], twR2[8], twA[8], twB[8], twC[8];
  {
    const int sA = t0 + 512 * w;
    const u32x4 xa = *(const u32x4*)(x + (size_t)sA * D + lane * 8), xb = *(const u32x4*)(x + (size_t)(sA + 256) * D + lane * 8);
    twid7(twR1, (float)lane * (1.0f / 512.0f));
    twid7(twR2, (float)(lane & 7) * (1.0f / 64.0f));
    *(LAS u32x4*)((LAS char*)wbuf + lane * 16) = xa;
    *(LAS u32x4*)((LAS char*)wbuf + 1024 + lane * 16) = xb;
    wave_sync();
    cf a[8];
#pragma unroll
    for (int j = 0; j < 8; ++j) a[j] = cf{bf_f(((LAS u16*)wbuf)[lane + 64 * j]), bf_f(((LAS u16*)wbuf)[512 + lane + 64 * j])};
    wave_sync();
    fft512_wave<0>(a, wbuf, lane, twR1, twR2);
    const int ub = (t0 >> 5) + 8 * ((t0 >> 2) & 7), vb = (t0 & 3) * 64 + w * 16;
    typedef unsigned u32x4_u __attribute__((ext_vector_type(4), aligned(2)));
    const size_t wofs = (size_t)(ub + 64 * (lane >> 1)) * DF + vb + (lane & 1) * 8;
    const u32x4 fr = *(const u32x4_u*)(w_r + wofs), fi = *(const u32x4_u*)(w_i + wofs);
    u16 wov[8] = {0, 0, 0, 0, 0, 0, 0, 0};
    if (t >= 128 && t < 136) {
      const int j = t - 128, u = ub + 128 * (t0 & 3) + 64 * (j >> 2) + 512 * (j & 3), un = (S - u) & (S - 1);
      wov[0] = w_r[u * DF]; wov[1] = w_i[u * DF]; wov[2] = w_r[un * DF]; wov[3] = w_i[un * DF];
      wov[4] = w_r[u * DF + 256]; wov[5] = w_i[u * DF + 256]; wov[6] = w_r[un * DF + 256]; wov[7] = w_i[un * DF + 256];
    }
    const int kl = (lane >> 3) + 8 * (lane & 7);
    wave_sync();
#pragma unroll
    for (int k = 0; k < 8; ++k) wbuf[k * 64 + kl] = a[k];
    lds_signal(SY(SY_F1A), lane);
    cf tw[8]; twid7(tw, (float)t0 * (1.0f / 2048.0f));
    constexpr float SC = 1.0f / 2097152.0f;
    lds_wait(SY(SY_F1A), 4);
    {
      const int mi = t ? 512 - t : 256;
#pragma unroll
      for (int q = 0; q < 4; ++q) {
        const cf A = buf[q * 576 + t] * SC, B = buf[q * 576 + mi] * SC;
        a[2 * q]     = t ? cf{A.x + B.x, A.y - B.y} : cf{2.f * A.x, 2.f * B.x};
        a[2 * q + 1] = t ? cf{A.y + B.y, B.x - A.x} : cf{2.f * A.y, 2.f * B.y};
      }
    }
    lds_signal(SY(SY_F1B), lane);
    dft8<0>(a);
#pragma unroll
    for (int k = 1; k < 8; ++k) a[k] = cmul(a[k], tw[k]);
    lds_wait(SY(SY_F1B), 4);
#pragma unroll
    for (int q = 0; q < 4; ++q) ((LAS f32x4*)wbuf)[lane * 4 + (q ^ ((lane >> 1) & 3))] = f32x4{a[2 * q].x, a[2 * q].y, a[2 * q + 1].x, a[2 * q + 1].y};
    wave_sync();
    u32x4 xs[4], wt2[2];
#pragma unroll
    for (int m = 0; m < 4; ++m) { const int p = m * 64 + lane; xs[m] = ((LAS u32x4*)wbuf)[p ^ ((p >> 3) & 3)]; }
    TS(1);
    wave_sync();
    {
      LAS char* st = (LAS char*)wbuf;
      LAS unsigned* d0 = (LAS unsigned*)(st + (lane >> 1) * 36 + (lane & 1) * 16);
#pragma unroll
      for (int i = 0; i < 4; ++i) { d0[i] = fr[i]; d0[288 + i] = fi[i]; }
      wave_sync();
#pragma unroll
      for (int n = 0; n < 2; ++n) {
        const LAS u16* sr = (const LAS u16*)(st + (lane & 7) * 36) + (lane >> 3) + 8 * n;
#pragma unroll
        for (int i = 0; i < 4; ++i) wt2[n][i] = (unsigned)sr[i * 144] | ((unsigned)sr[576 + i * 144] << 16);
      }
    }
    u32x4 wofr;
    {
      const cf h0 = {bf_f(wov[0]) + bf_f(wov[2]), bf_f(wov[1]) - bf_f(wov[3])}, h1 = {bf_f(wov[4]) + bf_f(wov[6]), bf_f(wov[5]) - bf_f(wov[7])};
      const cf P = (h0 + h1) * 0.25f, M = (h0 - h1) * 0.25f;
      wofr = __builtin_bit_cast(u32x4, f32x4{P.x, P.y, M.x, M.y});
    }
    twid7(twB, (float)in_ * (1.0f / 256.0f));
    twid7(twC, (float)ip * (1.0f / 32.0f));
    twid7(twA, (float)t * (1.0f / 2048.0f));
    pin8(twA); pin8(twB); pin8(twC); pin8(twR1); pin8(twR2);
    TS(2);
    XDELAY;
    {
#pragma unroll
      for (int m = 0; m < 4; ++m) st16_dev_s(ws + WS_X1 + (size_t)t0 * 16384 + m * 1024, w * 4096 + lane * 16, xs[m]);
#pragma unroll
      for (int n = 0; n < 2; ++n) st16_dev(ws + WS_WT + (size_t)(vb + (lane >> 3) + 8 * n) * WT_STRIDE + (t0 >> 2) * 128 + (lane & 7) * 16, wt2[n]);
      if (t >= 128 && t < 136) st16_dev(ws + WS_PM + ((t - 128) * 256 + t0) * 16, wofr);
    }
    wait_vm<0>();
    lds_signal(SY(SY_PUB2), lane);
  }
  TS(8);
  lds_wait(SY(SY_TAGOK), 1);
  const u32x2 tag = *(LAS u32x2*)(lds + L_SYNC + SY_TAG * 4);
  if (w == 0) {
    lds_wait(SY(SY_PUB2), 8);
    if (lane < 8) st8_dev(ws + WS_FL + 16384 + lane * 2048 + b * 8, tag);
    poll_all(ws + WS_FL + 16384 + xg * 2048, tag, lane);
    lds_signal(SY(SY_GO2), lane);
  }
  unsigned fbn = 0;
#define FFT_BAR() do { fbn += 4; lds_signal(SY(SY_FB), lane); lds_wait(SY(SY_FB), fbn); } while (0)
  {
    const int v = t0;
    LAS cf* bN = buf + k1n * 292 + in_;
    LAS cf* bO = buf + k1 * 292 + k2 * 36 + ip;
    LAS cf* bP = buf + k1 * 292 + k2 * 36 + ip * 8;
    LAS cf* bA = buf + (t >> 5) * 36 + (t & 31);
    lds_wait(SY(SY_GO2), 1);
    TS(3);
    cf a[8]; u32x4 wv0, wv1;
    {
      const unsigned xo = (unsigned)((in_ * 256 + v) * 64 + k1n * 8);
      const char* xb = ws + WS_X1;
#define LDX(j) asm volatile("global_load_dwordx2 %0, %1, %2 sc1" : "=&v"(a[j]) : "v"(xo), "s"(xb + (size_t)(j) * 524288) : "memory")
      LDX(0); LDX(4); LDX(1); LDX(5); LDX(2); LDX(6); LDX(3); LDX(7);
      const char* wt = ws + WS_WT + (size_t)v * WT_STRIDE;
      asm volatile("global_load_dwordx4 %0, %2, %3 sc1\n\tglobal_load_dwordx4 %1, %2, %3 offset:16 sc1" : "=&v"(wv0), "=&v"(wv1) : "v"(t * 32), "s"(wt) : "memory");
    }
    asm volatile("s_waitcnt vmcnt(0)" : "+v"(a[0]), "+v"(a[1]), "+v"(a[2]), "+v"(a[3]), "+v"(a[4]), "+v"(a[5]), "+v"(a[6]), "+v"(a[7]), "+v"(wv0), "+v"(wv1) :: "memory");
    f32x4 pm[8];
    if (v == 0) {
#define LDP(j) asm volatile("global_load_dwordx4 %0, %1, %2 sc1" : "=&v"(pm[j]) : "v"(t * 16), "s"(ws + WS_PM + (j) * 4096) : "memory")
      LDP(0); LDP(1); LDP(2); LDP(3); LDP(4); LDP(5); LDP(6); LDP(7);
    }
    dft8<0>(a);
#pragma unroll
    for (int k = 1; k < 8; ++k) a[k] = cmul(a[k], twB[k]);
#pragma unroll
    for (int k = 0; k < 8; ++k) bN[36 * k] = a[k];
    FFT_BAR();
#pragma unroll
    for (int j = 0; j < 8; ++j) a[j] = bO[4 * j];
    dft8<0>(a);
#pragma unroll
    for (int k = 1; k < 8; ++k) a[k] = cmul(a[k], twC[k]);
#pragma unroll
    for (int k = 0; k < 8; ++k) bO[4 * k] = a[k];
    wave_sync();
#pragma unroll
    for (int j = 0; j < 8; ++j) a[j] = bP[j];
    dft4<0>(a[0], a[1], a[2], a[3]);
    dft4<0>(a[4], a[5], a[6], a[7]);
    if (v != 0) {
#pragma unroll
      for (int j = 0; j < 4; ++j) { a[j] = cmul(a[j], cf{bf_lo(wv0[j]), bf_hi(wv0[j])}); a[4 + j] = cmul(a[4 + j], cf{bf_lo(wv1[j]), bf_hi(wv1[j])}); }
    } else {
#pragma unroll
      for (int j = 0; j < 8; ++j) bP[j] = a[j];
      asm volatile("s_waitcnt vmcnt(0)" : "+v"(pm[0]), "+v"(pm[1]), "+v"(pm[2]), "+v"(pm[3]), "+v"(pm[4]), "+v"(pm[5]), "+v"(pm[6]), "+v"(pm[7]) :: "memory");
      FFT_BAR();
#pragma unroll
      for (int j = 0; j < 8; ++j) {
        const int un = (S - (k1 + 8 * k2 + 64 * (2 * ip + (j >> 2)) + 512 * (j & 3))) & (S - 1);
        cf cn = buf[(un & 7) * 292 + ((un >> 3) & 7) * 36 + ((un >> 6) & 7) * 4 + (un >> 9)]; cn.y = -cn.y;
        a[j] = cmul(a[j], cf{pm[j].x, pm[j].y}) + cmul(cn, cf{pm[j].z, pm[j].w});
      }
      FFT_BAR();
    }
    dft4<1>(a[0], a[1], a[2], a[3]);
    dft4<1>(a[4], a[5], a[6], a[7]);
#pragma unroll
    for (int j = 0; j < 8; ++j) bP[j] = a[j];
    wave_sync();
#pragma unroll
    for (int k = 0; k < 8; ++k) a[k] = bO[4 * k];
#pragma unroll
    for (int k = 1; k < 8; ++k) a[k] = cmulc(a[k], twC[k]);
    dft8<1>(a);
#pragma unroll
    for (int j = 0; j < 8; ++j) bO[4 * j] = a[j];
    FFT_BAR();
#pragma unroll
    for (int k = 0; k < 8; ++k) a[k] = bN[36 * k];
#pragma unroll
    for (int k = 1; k < 8; ++k) a[k] = cmulc(a[k], twB[k]);
    dft8<1>(a);
#pragma unroll
    for (int j = 0; j < 8; ++j) bN[36 * j] = a[j];
    FFT_BAR();
#pragma unroll
    for (int k = 0; k < 8; ++k) a[k] = bA[292 * k];
#pragma unroll
    for (int k = 1; k < 8; ++k) a[k] = cmulc(a[k], twA[k]);
    dft8<1>(a);
#pragma unroll
    for (int j = 0; j < 8; ++j) bA[292 * j] = a[j];
    wave_sync();
    {
      const int te = t & ~1, jo = (t & 1) * 4;
      const unsigned zo = (unsigned)(((te >> 3) + jo * 32) * Z_STRIDE + (te & 7) * 8);
      const LAS cf* src = bA - (t & 1) + 292 * jo;
#pragma unroll
      for (int j = 0; j < 4; ++j) st16_dev_s(ws + WS_Z + (size_t)j * 32 * Z_STRIDE + v * 64, zo, *(const LAS u32x4*)(src + 292 * j));
    }
    {
      const char* of = ws + WS_O + ((size_t)(rb0 + 8 * (w >> 1)) * 32 + nb * 4) * 1024 + (w & 1) * 1024;
      st16_dev_s(of, lane * 16, u32x4{SENT, SENT, SENT, SENT}); st16_dev_s(of + 2048, lane * 16, u32x4{SENT, SENT, SENT, SENT});
    }
    wait_vm<0>();
    TS(4);
    if (lds_ticket(SY(SY_PUB3), lane) == 3) {
      if (lane < 8) st8_dev(ws + WS_FL + 32768 + lane * 2048 + b * 8, tag);
      poll_all(ws + WS_FL + 32768 + xg * 2048, tag, lane);
      lds_signal(SY(SY_GO3), lane);
    }
    lds_wait_slow(SY(SY_GO3), 1);
  }
  TS(5);
  {
    const int rbf = rb0 + 8 * (nb >> 2), sblk = rbf * 4 + (nb & 3);
    cf a[8];
    {
      const char* z = ws + WS_Z + (size_t)sblk * Z_STRIDE + w * 16;
      const unsigned o1 = lane * 64, o2 = ((64 - lane) & 63) * 64 + (lane ? 0 : 4096);
      f32x4 q[8];
      asm volatile("global_load_dwordx4 %0, %8, %10 sc1\n\tglobal_load_dwordx4 %1, %8, %11 sc1\n\tglobal_load_dwordx4 %2, %8, %12 sc1\n\tglobal_load_dwordx4 %3, %8, %13 sc1\n\t"
                   "global_load_dwordx4 %4, %9, %13 sc1\n\tglobal_load_dwordx4 %5, %9, %12 sc1\n\tglobal_load_dwordx4 %6, %9, %11 sc1\n\tglobal_load_dwordx4 %7, %9, %10 sc1\n\ts_waitcnt vmcnt(0)"
                   : "=&v"(q[0]), "=&v"(q[1]), "=&v"(q[2]), "=&v"(q[3]), "=&v"(q[4]), "=&v"(q[5]), "=&v"(q[6]), "=&v"(q[7])
                   : "v"(o1), "v"(o2), "s"(z), "s"(z + 4096), "s"(z + 8192), "s"(z + 12288) : "memory");
#pragma unroll
      for (int j = 0; j < 4; ++j) a[j] = cf{q[j].x - q[j].w, q[j].y + q[j].z};
#pragma unroll
      for (int j = 4; j < 8; ++j) a[j] = cf{q[j].x + q[j].w, q[j].z - q[j].y};
      if (lane == 0) { a[0] = cf{q[0].x, q[0].z}; a[4] = cf{q[0].y, q[0].w}; }
    }
    TS(9);
    fft512_wave<1>(a, wbuf, lane, twR1, twR2);
    asm volatile("" :: "v"(a[0]), "v"(a[7])); TS(10);
    const int kl = (lane >> 3) + 8 * (lane & 7);
    wave_sync();
    LAS u16* rows = (LAS u16*)wbuf;
#pragma unroll
    for (int k = 0; k < 8; ++k) {
      const unsigned p = pk_bf16(a[k].x, a[k].y);
      rows[kl + 64 * k] = (u16)p; rows[520 + kl + 64 * k] = (u16)(p >> 16);
    }
    wave_sync();
    {
      const int par = lane >> 5, nt = (lane >> 1) & 15, hh = lane & 1;
      const LAS u16* src = rows + par * 520 + nt * 32 + hh * 4;
      u32x2 q[4];
#pragma unroll
      for (int g = 0; g < 4; ++g) q[g] = *(const LAS u32x2*)(src + 8 * g);
      const char* bd = ws + WS_B + ((size_t)rbf * 1024 + (nb & 3) * 8 + w * 2) * 32;
      const unsigned bo = (unsigned)((nt * 64 + hh * 32 + par) * 32);
      st16_dev_s(bd, bo, u32x4{q[0][0], q[0][1], q[1][0], q[1][1]});
      st16_dev_s(bd + 16, bo, u32x4{q[2][0], q[2][1], q[3][0], q[3][1]});
    }
    TS(11);
    wait_vm<0>();
    TS(12);
    if (lds_ticket(SY(SY_PUB4), lane) == 3) {
      if (lane == 0) st8_dev(ws + WS_FL + 49152 + (xg * 4 + rm) * 256 + nb * 8, tag);
      const unsigned fo = (unsigned)((xg * 4 + rm) * 256 + (lane & 7) * 8);
      for (unsigned spin = 0; spin < SPIN_CAP; ++spin) {
        u32x2 f;
        asm volatile("global_load_dwordx2 %0, %1, %2 sc1\n\ts_waitcnt vmcnt(0)" : "=&v"(f) : "v"(fo), "s"(ws + WS_FL + 49152) : "memory");
        if (wave_all(f[0] == tag[0] && f[1] == tag[1])) break;
        __builtin_amdgcn_s_sleep(1);
      }
      lds_signal(SY(SY_GO4), lane);
    }
  }
  TS(13);
  {
    const int fr_ = w >> 1, fh = w & 1;
    const char* og = ws + WS_O + (size_t)(rb0 + 8 * fr_) * 32768;
    const unsigned dst = (fr_ ? L_V : L_K) + fh * 14 * 1024 + lane * 16;
    u32x4 f[14];
    unsigned o[7];
#pragma unroll
    for (int i = 0; i < 7; ++i) o[i] = (unsigned)(((nb * 4 + 4 + fh * 14 + 2 * i) & 31) * 1024 + lane * 16);
    lds_wait_slow(SY(SY_ODONE), 2);
    lds_wait(SY(SY_ODONE), 4);
    TS(14);
    for (unsigned spin = 0; spin < SPIN_CAP; ++spin) {
#define LD2(i) asm volatile("global_load_dwordx4 %0, %2, %3 sc1\n\tglobal_load_dwordx4 %1, %2, %3 offset:1024 sc1" : "=&v"(f[2 * i]), "=&v"(f[2 * i + 1]) : "v"(o[i]), "s"(og) : "memory")
      LD2(0); LD2(1); LD2(2); LD2(3); LD2(4); LD2(5); LD2(6);
      asm volatile("s_waitcnt vmcnt(0)" : "+v"(f[0]), "+v"(f[1]), "+v"(f[2]), "+v"(f[3]), "+v"(f[4]), "+v"(f[5]), "+v"(f[6]), "+v"(f[7]), "+v"(f[8]), "+v"(f[9]),
                   "+v"(f[10]), "+v"(f[11]), "+v"(f[12]), "+v"(f[13]) :: "memory");
      unsigned m = 0u;
#pragma unroll
      for (int i = 0; i < 14; ++i) m = umax(m, umax(umax(f[i][0], f[i][1]), umax(f[i][2], f[i][3])));
      if (wave_all(m != SENT)) break;
      __builtin_amdgcn_s_sleep(2);
    }
#pragma unroll
    for (int i = 0; i < 14; ++i) *(LAS u32x4*)(lds + dst + i * 1024) = f[i];
    lds_signal(SY(SY_OSH), lane);
    TS(15);
    TDUMP(2);
  }
}

extern "C" void kernel_launch(void* const* d_in, const int* in_sizes, int n_in, void* d_out, int out_size, void* d_ws, size_t ws_size,
                              hipStream_t stream) {
  (void)hipFuncSetAttribute((const void*)k_main, hipFuncAttributeMaxDynamicSharedMemorySize, LDS_BYTES);
  k_main<<<256, 768, LDS_BYTES, stream>>>((const u16*)d_in[0], (const u16*)d_in[1], (const u16*)d_in[2], (const u16*)d_in[3], (const u16*)d_in[4],
                                          (const u16*)d_in[6], (const u16*)d_in[7], (const u16*)d_in[8], (char*)d_ws, (u16*)d_out);
}
```
